```python
import torch

def _softmax(x, axis=-1):
    return torch.softmax(x, dim=axis)

def _gelu(x):
    return torch.nn.functional.gelu(x, approximate='tanh')

def _silu(x):
    return torch.nn.functional.silu(x)

def _sigmoid(x):
    return torch.sigmoid(x)

def _layernorm(x, axis=-1, eps=1e-05):
    m = x.mean(dim=axis, keepdim=True)
    v = x.var(dim=axis, keepdim=True, unbiased=False)
    return (x - m) / torch.sqrt(v + eps)

def _scatter_add(tgt, idx, src, axis=0):
    return tgt.clone().index_add_(axis, idx, src)

def _segment_sum(src, seg, n):
    out = torch.zeros((n,) + src.shape[1:], dtype=src.dtype)
    return out.index_add_(0, seg, src)

def _pad(x, pw):
    flat = tuple((v for pair in reversed(pw) for v in pair))
    return torch.nn.functional.pad(x, flat)
kernel_name = 'interleave_layers__grouped_kv_mqa__memory_aug_recurrent_mem_tr__prompt'

def setup_inputs():

    _w = lambda a: (lambda t: t.to(torch.bfloat16) if t.is_floating_point() else t)(torch.as_tensor(a))

    def _A_setup():
        B, S, D, H = (1, 2048, 512, 32)
        Dh = D // H
        return {'x': _w((lambda *s: torch.randn(*s, dtype=torch.bfloat16) * 0.05)(B, S, D)), 'wq': _w((lambda *s: torch.randn(*s, dtype=torch.bfloat16) * 0.05)(D, H * Dh)), 'wk': _w((lambda *s: torch.randn(*s, dtype=torch.bfloat16) * 0.05)(D, Dh)), 'wv': _w((lambda *s: torch.randn(*s, dtype=torch.bfloat16) * 0.05)(D, Dh)), 'wo': _w((lambda *s: torch.randn(*s, dtype=torch.bfloat16) * 0.05)(H * Dh, D))}
    _A_in = _A_setup()

    def _B_setup():
        B, S, Dm = (1, 2048, 512)
        H, Dh = (32, 128)
        M, seg = (8, 32)
        seg = min(seg, S)
        S = S // seg * seg
        return {'x': _w((lambda *s: torch.randn(*s, dtype=torch.bfloat16) * 0.05)(B, S, Dm)), 'mem0': _w((lambda *s: torch.randn(*s, dtype=torch.bfloat16) * 0.8)(B, M, Dm)), 'Wq': _w((lambda *s: torch.randn(*s, dtype=torch.bfloat16) * 1.6)(Dm, H, Dh) * Dm ** (-0.5)), 'Wk': _w((lambda *s: torch.randn(*s, dtype=torch.bfloat16) * 1.6)(Dm, H, Dh) * Dm ** (-0.5)), 'Wv': _w((lambda *s: torch.randn(*s, dtype=torch.bfloat16) * 0.8)(Dm, H, Dh) * Dm ** (-0.5)), 'Wo': _w((lambda *s: torch.randn(*s, dtype=torch.bfloat16) * 0.8)(H, Dh, Dm) * (H * Dh) ** (-0.5)), 'seg_len': seg}
    _B_in = _B_setup()
    return {'A_x': _A_in['x'], 'A_wq': _A_in['wq'], 'A_wk': _A_in['wk'], 'A_wv': _A_in['wv'], 'A_wo': _A_in['wo'], 'B_x': _B_in['x'], 'B_mem0': _B_in['mem0'], 'B_Wq': _B_in['Wq'], 'B_Wk': _B_in['Wk'], 'B_Wv': _B_in['Wv'], 'B_Wo': _B_in['Wo'], 'B_seg_len': _B_in['seg_len']}

def reference(A_x, A_wq, A_wk, A_wv, A_wo, B_x, B_mem0, B_Wq, B_Wk, B_Wv, B_Wo, B_seg_len):

    def _A_ref(x, wq, wk, wv, wo):
        B, S, D = x.shape
        H = 32
        Dh = D // H
        q = (lambda x, *d: x.permute(*d))((x @ wq).reshape(B, S, H, Dh), 0, 2, 1, 3)
        k = x @ wk
        v = x @ wv
        scores = torch.einsum('bhqd,bkd->bhqk', q, k) * Dh ** (-0.5)
        p = _softmax(scores, axis=-1)
        o = torch.einsum('bhqk,bkd->bhqd', p, v)
        return (lambda x, *d: x.permute(*d))(o, 0, 2, 1, 3).reshape(B, S, H * Dh) @ wo

    def _B_ref(x, mem0, Wq, Wk, Wv, Wo, seg_len):
        B, S, Dm = x.shape
        H, Dh = (Wq.shape[1], Wq.shape[2])
        M = mem0.shape[1]
        scale = Dh ** (-0.5)
        n_seg = S // seg_len
        L = M + seg_len
        idx = torch.arange(L)
        mask = (idx[:, None] < M) | (idx[:, None] >= idx[None, :])
        mem = mem0
        outs = []
        for s in range(n_seg):
            xs = x[:, s * seg_len:(s + 1) * seg_len, :].clone()
            h = torch.concatenate([mem, xs], axis=1)
            q = torch.einsum('btd,dhe->bhte', h, Wq)
            k = torch.einsum('btd,dhe->bhte', h, Wk)
            v = torch.einsum('btd,dhe->bhte', h, Wv)
            scores = torch.einsum('bhqe,bhke->bhqk', q, k) * scale
            scores = torch.where(mask[None, None], scores, -1000000000.0)
            p = _softmax(scores, axis=-1)
            o = torch.einsum('bhqk,bhke->bhqe', p, v)
            o = torch.einsum('bhte,hed->btd', o, Wo)
            mem = o[:, :M, :].clone()
            outs.append(o[:, M:, :])
        return torch.concatenate(outs, axis=1)
    x = A_x
    x = x + _A_ref(_layernorm(x, axis=-1), A_wq, A_wk, A_wv, A_wo)
    x = x + _B_ref(_layernorm(x, axis=-1), B_mem0, B_Wq, B_Wk, B_Wv, B_Wo, B_seg_len)
    x = x + _A_ref(_layernorm(x, axis=-1), A_wq, A_wk, A_wv, A_wo)
    x = x + _B_ref(_layernorm(x, axis=-1), B_mem0, B_Wq, B_Wk, B_Wv, B_Wo, B_seg_len)
    return x
```

```cpp
#include <hip/hip_runtime.h>
#include <hip/hip_bf16.h>
#include <stdint.h>

typedef unsigned short u16;
typedef __attribute__((ext_vector_type(8))) __bf16 bf16x8;
typedef __attribute__((ext_vector_type(4))) __bf16 bf16x4;
typedef __attribute__((ext_vector_type(4))) float f32x4;
typedef __attribute__((ext_vector_type(16))) float f32x16;
typedef __attribute__((ext_vector_type(4))) unsigned u32x4;
typedef __attribute__((ext_vector_type(2))) unsigned u32x2;

namespace {

constexpr int S_LEN = 2048, DM = 512;
constexpr int NSEG = 64, SEG = 32, MEMR = 8;
constexpr int HB = 32, DHB = 128, NB = HB * DHB;
constexpr int QKVA_LD = 640;

__device__ __forceinline__ float bf2f(u16 v) { return __uint_as_float(((unsigned)v) << 16); }
__device__ __forceinline__ u16 f2bf(float f) { __bf16 b = (__bf16)f; return __builtin_bit_cast(u16, b); }
__device__ __forceinline__ float rbf(float f) { return bf2f(f2bf(f)); }
__device__ __forceinline__ unsigned pk2(float a, float b) { return (unsigned)f2bf(a) | ((unsigned)f2bf(b) << 16); }

__device__ __forceinline__ float xsum16_32(float x) {
    auto a = __builtin_amdgcn_permlane16_swap(__float_as_uint(x), __float_as_uint(x), false, false);
    x = __uint_as_float(a[0]) + __uint_as_float(a[1]);
    auto b = __builtin_amdgcn_permlane32_swap(__float_as_uint(x), __float_as_uint(x), false, false);
    return __uint_as_float(b[0]) + __uint_as_float(b[1]);
}
__device__ __forceinline__ float xmax16_32(float x) {
    auto a = __builtin_amdgcn_permlane16_swap(__float_as_uint(x), __float_as_uint(x), false, false);
    x = fmaxf(__uint_as_float(a[0]), __uint_as_float(a[1]));
    auto b = __builtin_amdgcn_permlane32_swap(__float_as_uint(x), __float_as_uint(x), false, false);
    return fmaxf(__uint_as_float(b[0]), __uint_as_float(b[1]));
}

struct TJob { const u16* src; u16* dst; int R; int C; int ldd; int tile0; };

__global__ __launch_bounds__(256) void prep_kernel(const u16* bq, const u16* bk, const u16* bv, const u16* bo,
                                                    const u16* aq, const u16* ak, const u16* av, const u16* ao,
                                                    u16* wtB, u16* wtoB, u16* wtA, u16* wtoA) {
    __shared__ u16 tile[64][72];
    int t = blockIdx.x;
    const u16* src; u16* dst; int R, C, ldd;
    if (t < 1536)      { int j = t >> 9; t &= 511; src = j == 0 ? bq : (j == 1 ? bk : bv); dst = wtB + (size_t)j * 4096 * 512; R = 512; C = 4096; ldd = 512; }
    else if (t < 2048) { t -= 1536; src = bo; dst = wtoB; R = 4096; C = 512; ldd = 4096; }
    else if (t < 2112) { t -= 2048; src = aq; dst = wtA; R = 512; C = 512; ldd = 512; }
    else if (t < 2176) { t -= 2112; src = ao; dst = wtoA; R = 512; C = 512; ldd = 512; }
    else if (t < 2184) { t -= 2176; src = ak; dst = wtA + 512 * 512; R = 512; C = 16; ldd = 512; }
    else if (t < 2192) { t -= 2184; src = av; dst = wtA + 528 * 512; R = 512; C = 16; ldd = 512; }
    else {
        u32x4 z = {0u, 0u, 0u, 0u};
        u32x4* p = (u32x4*)(wtA + 544 * 512);
        for (int i = threadIdx.x; i < 96 * 512 / 8; i += 256) p[i] = z;
        return;
    }
    const int tc = (C + 63) >> 6;
    const int r0 = (t / tc) * 64, c0 = (t % tc) * 64;
    const int tid = threadIdx.x;
    for (int i = 0; i < 2; ++i) {
        const int r = (tid >> 3) + 32 * i, ch = tid & 7;
        const int c = c0 + ch * 8;
        if (c < C) {
            u32x4 v = *(const u32x4*)(src + (size_t)(r0 + r) * C + c);
            u16* d = &tile[r][ch * 8];
            d[0] = (u16)v.x; d[1] = (u16)(v.x >> 16); d[2] = (u16)v.y; d[3] = (u16)(v.y >> 16);
            d[4] = (u16)v.z; d[5] = (u16)(v.z >> 16); d[6] = (u16)v.w; d[7] = (u16)(v.w >> 16);
        }
    }
    __syncthreads();
    for (int i = 0; i < 2; ++i) {
        const int c = (tid >> 3) + 32 * i, ch = tid & 7;
        if (c0 + c < C) {
            u32x4 v;
            v.x = (unsigned)tile[ch * 8 + 0][c] | ((unsigned)tile[ch * 8 + 1][c] << 16);
            v.y = (unsigned)tile[ch * 8 + 2][c] | ((unsigned)tile[ch * 8 + 3][c] << 16);
            v.z = (unsigned)tile[ch * 8 + 4][c] | ((unsigned)tile[ch * 8 + 5][c] << 16);
            v.w = (unsigned)tile[ch * 8 + 6][c] | ((unsigned)tile[ch * 8 + 7][c] << 16);
            *(u32x4*)(dst + (size_t)(c0 + c) * ldd + r0 + ch * 8) = v;
        }
    }
}

__global__ __launch_bounds__(256) void ln_kernel(const u16* __restrict__ x, u16* __restrict__ xn, u32x4* xchg, int xchg_vec) {
    const int tid = threadIdx.x, w = tid >> 6, lane = tid & 63;
    const int row = blockIdx.x * 4 + w;
    if (xchg != nullptr) {
        const u32x4 pz = {0xFFFFFFFFu, 0xFFFFFFFFu, 0xFFFFFFFFu, 0xFFFFFFFFu};
        for (int i = blockIdx.x * 256 + tid; i < xchg_vec; i += gridDim.x * 256) xchg[i] = pz;
    }
    u32x4 v = *(const u32x4*)(x + (size_t)row * DM + lane * 8);
    float f[8];
    f[0] = bf2f((u16)v.x); f[1] = bf2f((u16)(v.x >> 16)); f[2] = bf2f((u16)v.y); f[3] = bf2f((u16)(v.y >> 16));
    f[4] = bf2f((u16)v.z); f[5] = bf2f((u16)(v.z >> 16)); f[6] = bf2f((u16)v.w); f[7] = bf2f((u16)(v.w >> 16));
    float s = 0.f;
#pragma unroll
    for (int i = 0; i < 8; ++i) s += f[i];
#pragma unroll
    for (int o = 32; o >= 1; o >>= 1) s += __shfl_xor(s, o);
    const float mean = s * (1.0f / DM);
    float q = 0.f;
#pragma unroll
    for (int i = 0; i < 8; ++i) { const float d = f[i] - mean; q += d * d; }
#pragma unroll
    for (int o = 32; o >= 1; o >>= 1) q += __shfl_xor(q, o);
    const float var = q * (1.0f / DM);
    const float mb = rbf(mean);
    const float den = rbf(sqrtf(rbf(rbf(var) + 1e-5f)));
    u32x4 r;
    float y[8];
#pragma unroll
    for (int i = 0; i < 8; ++i) y[i] = rbf(f[i] - mb) / den;
    r.x = pk2(y[0], y[1]); r.y = pk2(y[2], y[3]); r.z = pk2(y[4], y[5]); r.w = pk2(y[6], y[7]);
    *(u32x4*)(xn + (size_t)row * DM + lane * 8) = r;
}

template <int BM, int BN, int EPI>
__global__ __launch_bounds__(256, 2) void gemm_nt(const u16* __restrict__ A, int lda, const u16* __restrict__ Bt, int ldb,
                                                   u16* __restrict__ C, int ldc, const u16* __restrict__ R, int ldr, int K, int ntn) {
    constexpr int MT = BM / 32, NT = BN / 32;
    constexpr int LA = BM * 8 / 256, LB = BN * 8 / 256;
    __shared__ u32x4 sA[2][BM * 8];
    __shared__ u32x4 sB[2][BN * 8];
    const int tid = threadIdx.x, w = tid >> 6, lane = tid & 63, g = lane >> 4, r16 = lane & 15;
    const int wm = w >> 1, wn = w & 1;
    const int bid = blockIdx.x;
    const int bn = (bid % ntn) * BN, bm = (bid / ntn) * BM;
    f32x4 acc[NT][MT];
#pragma unroll
    for (int i = 0; i < NT; ++i)
#pragma unroll
        for (int j = 0; j < MT; ++j) acc[i][j] = (f32x4){0.f, 0.f, 0.f, 0.f};
    u32x4 ra[LA], rb[LB];
    auto gload = [&](int k0) {
#pragma unroll
        for (int i = 0; i < LA; ++i) { const int idx = tid + 256 * i, row = idx >> 3, c = idx & 7; ra[i] = *(const u32x4*)(A + (size_t)(bm + row) * lda + k0 + c * 8); }
#pragma unroll
        for (int i = 0; i < LB; ++i) { const int idx = tid + 256 * i, row = idx >> 3, c = idx & 7; rb[i] = *(const u32x4*)(Bt + (size_t)(bn + row) * ldb + k0 + c * 8); }
    };
    auto sstore = [&](int buf) {
#pragma unroll
        for (int i = 0; i < LA; ++i) { const int idx = tid + 256 * i, row = idx >> 3, c = idx & 7; sA[buf][row * 8 + (c ^ ((row >> 1) & 7))] = ra[i]; }
#pragma unroll
        for (int i = 0; i < LB; ++i) { const int idx = tid + 256 * i, row = idx >> 3, c = idx & 7; sB[buf][row * 8 + (c ^ ((row >> 1) & 7))] = rb[i]; }
    };
    const int nst = K >> 6;
    gload(0); sstore(0); __syncthreads();
    for (int st = 0; st < nst; ++st) {
        const int buf = st & 1;
        if (st + 1 < nst) gload((st + 1) << 6);
#pragma unroll
        for (int ks = 0; ks < 2; ++ks) {
            bf16x8 fa[MT], fb[NT];
#pragma unroll
            for (int j = 0; j < MT; ++j) { const int row = wm * (BM / 2) + j * 16 + r16, c = ks * 4 + g; fa[j] = __builtin_bit_cast(bf16x8, sA[buf][row * 8 + (c ^ ((row >> 1) & 7))]); }
#pragma unroll
            for (int i = 0; i < NT; ++i) { const int row = wn * (BN / 2) + i * 16 + r16, c = ks * 4 + g; fb[i] = __builtin_bit_cast(bf16x8, sB[buf][row * 8 + (c ^ ((row >> 1) & 7))]); }
#pragma unroll
            for (int i = 0; i < NT; ++i)
#pragma unroll
                for (int j = 0; j < MT; ++j) acc[i][j] = __builtin_amdgcn_mfma_f32_16x16x32_bf16(fb[i], fa[j], acc[i][j], 0, 0, 0);
        }
        if (st + 1 < nst) sstore(buf ^ 1);
        __syncthreads();
    }
#pragma unroll
    for (int i = 0; i < NT; ++i)
#pragma unroll
        for (int j = 0; j < MT; ++j) {
            const int m = bm + wm * (BM / 2) + j * 16 + r16;
            const int n = bn + wn * (BN / 2) + i * 16 + 4 * g;
            f32x4 v = acc[i][j];
            u32x2 o;
            if (EPI == 1) {
                const u32x2 rr = *(const u32x2*)(R + (size_t)m * ldr + n);
                o.x = pk2(bf2f((u16)rr.x) + rbf(v[0]), bf2f((u16)(rr.x >> 16)) + rbf(v[1]));
                o.y = pk2(bf2f((u16)rr.y) + rbf(v[2]), bf2f((u16)(rr.y >> 16)) + rbf(v[3]));
            } else {
                o.x = pk2(v[0], v[1]); o.y = pk2(v[2], v[3]);
            }
            *(u32x2*)(C + (size_t)m * ldc + n) = o;
        }
}

constexpr int AVROW = S_LEN * 2 + 16;
__global__ __launch_bounds__(512, 2) void attnA_kernel(const u16* __restrict__ qkv, u16* __restrict__ o) {
    extern __shared__ __attribute__((aligned(16))) unsigned char smem[];
    unsigned char* sK = smem;
    unsigned char* sV = smem + 65536;
    unsigned char* sC = sV + 16 * AVROW;
    const int tid = threadIdx.x, w = tid >> 6, lane = tid & 63;
    for (int i = tid; i < S_LEN * 2; i += 512) {
        const int key = i >> 1, hf = i & 1;
        *(u32x4*)(sK + key * 32 + hf * 16) = *(const u32x4*)(qkv + (size_t)key * QKVA_LD + 512 + hf * 8);
    }
    for (int key = tid; key < S_LEN; key += 512) {
        const u32x4 v0 = *(const u32x4*)(qkv + (size_t)key * QKVA_LD + 528);
        const u32x4 v1 = *(const u32x4*)(qkv + (size_t)key * QKVA_LD + 536);
        const int kk = key & 31;
        const int pos = (key & ~31) + (kk & 16) + ((kk >> 2) & 1) * 8 + ((kk >> 3) & 1) * 4 + (kk & 3);
        const unsigned vv[8] = {v0.x, v0.y, v0.z, v0.w, v1.x, v1.y, v1.z, v1.w};
#pragma unroll
        for (int d = 0; d < 8; ++d) {
            *(u16*)(sV + (2 * d) * AVROW + pos * 2) = (u16)vv[d];
            *(u16*)(sV + (2 * d + 1) * AVROW + pos * 2) = (u16)(vv[d] >> 16);
        }
    }
    if (tid < 32) ((u16*)sC)[tid] = 0x3F80;
    else if (tid < 64) ((u16*)sC)[tid] = 0;
    __syncthreads();

    const int token = blockIdx.x * 8 + w;
    const int hh = lane >> 5, l32 = lane & 31;
    const bf16x8 qf = __builtin_bit_cast(bf16x8, *(const u32x4*)(qkv + (size_t)token * QKVA_LD + l32 * 16 + hh * 8));
    f32x16 O;
#pragma unroll
    for (int i = 0; i < 16; ++i) O[i] = 0.f;
    float mrun = -INFINITY;
    const float cs = 0.25f * 1.4426950408889634f;
    const int row = l32;
    const unsigned char* vbase = (row < 16) ? (sV + row * AVROW + hh * 16) : (sC + (row == 16 ? 0 : 64));
    const int vstep = (row < 16) ? 64 : 0;
    const int vhalf = (row < 16) ? 32 : 0;
    for (int t = 0; t < S_LEN / 32; ++t) {
        const bf16x8 kf = __builtin_bit_cast(bf16x8, *(const u32x4*)(sK + (t * 32 + l32) * 32 + hh * 16));
        f32x16 Z;
#pragma unroll
        for (int i = 0; i < 16; ++i) Z[i] = 0.f;
        f32x16 Sx = __builtin_amdgcn_mfma_f32_32x32x16_bf16(kf, qf, Z, 0, 0, 0);
        float mx = Sx[0];
#pragma unroll
        for (int i = 1; i < 16; ++i) mx = fmaxf(mx, Sx[i]);
        { auto rr = __builtin_amdgcn_permlane32_swap(__float_as_uint(mx), __float_as_uint(mx), false, false); mx = fmaxf(__uint_as_float(rr[0]), __uint_as_float(rr[1])); }
        if (!__all((mx - mrun) * cs <= 8.0f)) {
            const float mnew = fmaxf(mrun, mx);
            const float alpha = exp2f((mrun - mnew) * cs);
#pragma unroll
            for (int i = 0; i < 16; ++i) O[i] *= alpha;
            mrun = mnew;
        }
        const float mc = mrun * cs;
        bf16x8 p0, p1;
#pragma unroll
        for (int i = 0; i < 8; ++i) { p0[i] = (__bf16)exp2f(__builtin_fmaf(Sx[i], cs, -mc)); p1[i] = (__bf16)exp2f(__builtin_fmaf(Sx[8 + i], cs, -mc)); }
        const bf16x8 vf0 = __builtin_bit_cast(bf16x8, *(const u32x4*)(vbase + t * vstep));
        const bf16x8 vf1 = __builtin_bit_cast(bf16x8, *(const u32x4*)(vbase + t * vstep + vhalf));
        O = __builtin_amdgcn_mfma_f32_32x32x16_bf16(vf0, p0, O, 0, 0, 0);
        O = __builtin_amdgcn_mfma_f32_32x32x16_bf16(vf1, p1, O, 0, 0, 0);
    }
    float lsum = O[8];
    { auto rr = __builtin_amdgcn_permlane32_swap(__float_as_uint(lsum), __float_as_uint(lsum), false, false); lsum = __uint_as_float(rr[0]); }
    const float inv = 1.0f / lsum;
    u32x2 a, b;
    a.x = pk2(O[0] * inv, O[1] * inv); a.y = pk2(O[2] * inv, O[3] * inv);
    b.x = pk2(O[4] * inv, O[5] * inv); b.y = pk2(O[6] * inv, O[7] * inv);
    u16* op = o + (size_t)token * DM + l32 * 16;
    *(u32x2*)(op + hh * 4) = a;
    *(u32x2*)(op + 8 + hh * 4) = b;
}

#define POISON 0xFFFFFFFFu
constexpr int CH_WG = 64;
constexpr int PB_WORDS = 2 * CH_WG * CH_WG * 64;
constexpr int MB_WORDS = 3 * CH_WG * 32;
#define CH_D0 14
__device__ __forceinline__ void ld16_sc1(u32x4& v, const unsigned* p) { asm volatile("global_load_dwordx4 %0, %1, off sc1" : "=&v"(v) : "v"(p) : "memory"); }
__device__ __forceinline__ void st16_sc1(unsigned* p, u32x4 v) { asm volatile("global_store_dwordx4 %0, %1, off sc1\n\ts_nop 1" :: "v"(p), "v"(v) : "memory"); }
__device__ __forceinline__ void st8_sc1(unsigned* p, u32x2 v) { asm volatile("global_store_dwordx2 %0, %1, off sc1\n\ts_nop 1" :: "v"(p), "v"(v) : "memory"); }
#define WAITV() asm volatile("s_waitcnt vmcnt(0)" ::: "memory")
__device__ __forceinline__ bool okv(const u32x4& v) { return (v.x != POISON) & (v.y != POISON) & (v.z != POISON) & (v.w != POISON); }
__device__ __forceinline__ unsigned unpoison(unsigned u) { return u == POISON ? 0x7FC00000u : u; }
__device__ __forceinline__ void sleep_units(int n) { for (int i = 0; i < n; ++i) __builtin_amdgcn_s_sleep(1); }

__global__ __launch_bounds__(256, 1) void chain_kernel(const u16* __restrict__ WT, const u16* __restrict__ WTo,
                                                        const u16* __restrict__ QK, const u16* __restrict__ VT, const u16* __restrict__ mem0,
                                                        u32x4* __restrict__ KM, u32x2* __restrict__ VM, unsigned* PB, unsigned* MB, unsigned* abortw) {
    __shared__ u32x4 memf[16 * 64];
    __shared__ f32x4 sS[4][3][64];
    __shared__ u32x4 ofr[2][64];
    __shared__ f32x4 red[4][16];
    const int tid = threadIdx.x, w = tid >> 6, lane = tid & 63, g = lane >> 4, r16 = lane & 15;
    const int b = blockIdx.x, h = b >> 1, a = b & 1;
    const int u2 = w >> 1, e2 = w & 1;
    bf16x8 wq[2][16], wk[2][16], wv[16], wo[8][2];
#pragma unroll
    for (int tt = 0; tt < 2; ++tt) {
        const int d = 32 * w + 8 * (r16 >> 2) + 4 * tt + (r16 & 3);
        const u16* pq = WT + (size_t)(h * DHB + d) * DM + 8 * g;
        const u16* pk = pq + (size_t)NB * DM;
#pragma unroll
        for (int ks = 0; ks < 16; ++ks) {
            wq[tt][ks] = __builtin_bit_cast(bf16x8, *(const u32x4*)(pq + 32 * ks));
            wk[tt][ks] = __builtin_bit_cast(bf16x8, *(const u32x4*)(pk + 32 * ks));
        }
    }
    const int dvl = 32 * u2 + 8 * (r16 >> 2) + 4 * e2 + (r16 & 3);
    {
        const u16* pv = WT + (size_t)(2 * NB + h * DHB + 64 * a + dvl) * DM + 8 * g;
#pragma unroll
        for (int ks = 0; ks < 16; ++ks) wv[ks] = __builtin_bit_cast(bf16x8, *(const u32x4*)(pv + 32 * ks));
    }
#pragma unroll
    for (int t = 0; t < 8; ++t) {
        const u16* po = WTo + (size_t)(128 * w + 16 * t + r16) * NB + h * DHB + 64 * a + 8 * g;
        wo[t][0] = __builtin_bit_cast(bf16x8, *(const u32x4*)(po));
        wo[t][1] = __builtin_bit_cast(bf16x8, *(const u32x4*)(po + 32));
    }
    for (int i = tid; i < 1024; i += 256) memf[i] = (u32x4){0u, 0u, 0u, 0u};
    __syncthreads();
    for (int i = tid; i < 512; i += 256) {
        const int u = i >> 3, m = i & 7;
        memf[(u >> 2) * 64 + (u & 3) * 16 + m] = *(const u32x4*)(mem0 + m * DM + 8 * u);
    }
    const int keyr = 8 * (r16 >> 2) + (r16 & 3);
    const u16* kbase = QK + (size_t)keyr * 8192 + NB + h * DHB + 32 * w + 8 * g;
    const u16* vbase = VT + (size_t)(h * DHB + 64 * a + dvl) * S_LEN + 8 * g;
    u32x4 kt0 = *(const u32x4*)(kbase), kt1 = *(const u32x4*)(kbase + (size_t)4 * 8192), vt = *(const u32x4*)(vbase);
    __syncthreads();

    const float cs = 0.08838834764831845f * 1.4426950408889634f;
    bool dead = false;
    for (int s = 0; s < NSEG; ++s) {
        f32x4 aq0 = {0.f, 0.f, 0.f, 0.f}, aq1 = aq0, ak0 = aq0, ak1 = aq0, av = aq0;
#pragma unroll
        for (int ks = 0; ks < 16; ++ks) {
            const bf16x8 mf = __builtin_bit_cast(bf16x8, memf[ks * 64 + lane]);
            aq0 = __builtin_amdgcn_mfma_f32_16x16x32_bf16(wq[0][ks], mf, aq0, 0, 0, 0);
            aq1 = __builtin_amdgcn_mfma_f32_16x16x32_bf16(wq[1][ks], mf, aq1, 0, 0, 0);
            ak0 = __builtin_amdgcn_mfma_f32_16x16x32_bf16(wk[0][ks], mf, ak0, 0, 0, 0);
            ak1 = __builtin_amdgcn_mfma_f32_16x16x32_bf16(wk[1][ks], mf, ak1, 0, 0, 0);
            av = __builtin_amdgcn_mfma_f32_16x16x32_bf16(mf, wv[ks], av, 0, 0, 0);
        }
        bf16x8 qf, kmf, vmf;
#pragma unroll
        for (int i = 0; i < 4; ++i) { qf[i] = (__bf16)aq0[i]; qf[4 + i] = (__bf16)aq1[i]; kmf[i] = (__bf16)ak0[i]; kmf[4 + i] = (__bf16)ak1[i]; vmf[i] = (__bf16)av[i]; vmf[4 + i] = (__bf16)0.f; }
        const f32x4 z4 = {0.f, 0.f, 0.f, 0.f};
        sS[w][0][lane] = __builtin_amdgcn_mfma_f32_16x16x32_bf16(__builtin_bit_cast(bf16x8, kt0), qf, z4, 0, 0, 0);
        sS[w][1][lane] = __builtin_amdgcn_mfma_f32_16x16x32_bf16(__builtin_bit_cast(bf16x8, kt1), qf, z4, 0, 0, 0);
        sS[w][2][lane] = __builtin_amdgcn_mfma_f32_16x16x32_bf16(kmf, qf, z4, 0, 0, 0);
        if (a == 0) KM[((size_t)(s * HB + h) * 4 + w) * 64 + lane] = __builtin_bit_cast(u32x4, kmf);
        { const u32x4 vm4 = __builtin_bit_cast(u32x4, vmf); VM[(((size_t)(s * HB + h) * 2 + a) * 4 + w) * 64 + lane] = (u32x2){vm4.x, vm4.y}; }
        const u32x4 vtc = vt;
        if (s + 1 < NSEG) {
            kt0 = *(const u32x4*)(kbase + (size_t)(s + 1) * SEG * 8192);
            kt1 = *(const u32x4*)(kbase + (size_t)((s + 1) * SEG + 4) * 8192);
            vt = *(const u32x4*)(vbase + (size_t)(s + 1) * SEG);
        }
        __syncthreads();
        float sc[12];
#pragma unroll
        for (int tI = 0; tI < 3; ++tI) {
            const f32x4 p0 = sS[0][tI][lane], p1 = sS[1][tI][lane], p2 = sS[2][tI][lane], p3 = sS[3][tI][lane];
#pragma unroll
            for (int i = 0; i < 4; ++i) sc[tI * 4 + i] = ((p0[i] + p1[i]) + p2[i]) + p3[i];
        }
        if (g >= 2) { sc[8] = -INFINITY; sc[9] = -INFINITY; sc[10] = -INFINITY; sc[11] = -INFINITY; }
        float mx = sc[0];
#pragma unroll
        for (int i = 1; i < 12; ++i) mx = fmaxf(mx, sc[i]);
        mx = xmax16_32(mx);
        const float mc = mx * cs;
        float sum = 0.f;
#pragma unroll
        for (int i = 0; i < 12; ++i) { sc[i] = exp2f(__builtin_fmaf(sc[i], cs, -mc)); sum += sc[i]; }
        sum = xsum16_32(sum);
        const float inv = 1.0f / sum;
        bf16x8 pf0, pf1;
#pragma unroll
        for (int i = 0; i < 4; ++i) { pf0[i] = (__bf16)(sc[i] * inv); pf0[4 + i] = (__bf16)(sc[4 + i] * inv); pf1[i] = (__bf16)(sc[8 + i] * inv); pf1[4 + i] = (__bf16)0.f; }
        f32x4 ao = __builtin_amdgcn_mfma_f32_16x16x32_bf16(__builtin_bit_cast(bf16x8, vtc), pf0, z4, 0, 0, 0);
        ao = __builtin_amdgcn_mfma_f32_16x16x32_bf16(vmf, pf1, ao, 0, 0, 0);
        ((u32x2*)&ofr[u2][lane])[e2] = (u32x2){pk2(ao[0], ao[1]), pk2(ao[2], ao[3])};
        __syncthreads();
        const bf16x8 of0 = __builtin_bit_cast(bf16x8, ofr[0][lane]), of1 = __builtin_bit_cast(bf16x8, ofr[1][lane]);
        const int par = s & 1;
        {
            unsigned* pbase = PB + ((size_t)par * CH_WG * CH_WG + b) * 64 + (g & 1) * 32 + r16 * 4;
#pragma unroll
            for (int t = 0; t < 8; ++t) {
                f32x4 ac = __builtin_amdgcn_mfma_f32_16x16x32_bf16(wo[t][0], of0, z4, 0, 0, 0);
                ac = __builtin_amdgcn_mfma_f32_16x16x32_bf16(wo[t][1], of1, ac, 0, 0, 0);
                if (r16 < 8) {
                    const u32x4 v = {unpoison(__float_as_uint(ac[0])), unpoison(__float_as_uint(ac[1])), unpoison(__float_as_uint(ac[2])), unpoison(__float_as_uint(ac[3]))};
                    const int ured = 16 * w + 2 * t + (g >> 1);
                    st16_sc1(pbase + (size_t)ured * CH_WG * 64, v);
                }
            }
        }
        float r0 = 0.f, r1 = 0.f, r2 = 0.f, r3 = 0.f;
        const unsigned* gb = PB + ((size_t)par * CH_WG + b) * CH_WG * 64 + (size_t)(tid >> 4) * 64 + (tid & 15) * 4;
        {
            sleep_units(CH_D0);
            unsigned spins = 0;
            for (;;) {
                u32x4 v[4];
#pragma unroll
                for (int j = 0; j < 4; ++j) ld16_sc1(v[j], gb + (size_t)j * 16 * 64);
                WAITV();
                bool ok = true;
#pragma unroll
                for (int j = 0; j < 4; ++j) { asm volatile("" : "+v"(v[j])); ok &= okv(v[j]); }
                if (__all(ok)) {
#pragma unroll
                    for (int j = 0; j < 4; ++j) { r0 += __uint_as_float(v[j].x); r1 += __uint_as_float(v[j].y); r2 += __uint_as_float(v[j].z); r3 += __uint_as_float(v[j].w); }
                    break;
                }
                if (++spins > (1u << 18)) { __hip_atomic_store((__attribute__((address_space(1))) unsigned*)abortw, 1u, __ATOMIC_RELAXED, __HIP_MEMORY_SCOPE_AGENT); dead = true; break; }
                if ((spins & 63) == 0 && __hip_atomic_load((__attribute__((address_space(1))) unsigned*)abortw, __ATOMIC_RELAXED, __HIP_MEMORY_SCOPE_AGENT)) { dead = true; break; }
            }
        }
        r0 = xsum16_32(r0); r1 = xsum16_32(r1); r2 = xsum16_32(r2); r3 = xsum16_32(r3);
        if (lane < 16) red[w][lane] = (f32x4){r0, r1, r2, r3};
        WAITV();
        __syncthreads();
        if (w == 0 && lane < 32) {
            const int q = lane & 15;
            const f32x4 t0 = red[0][q], t1 = red[1][q], t2 = red[2][q], t3 = red[3][q];
            const int h2 = q >> 3, m = q & 7;
            if (lane < 16) {
                unsigned w0 = pk2(((t0[0] + t1[0]) + t2[0]) + t3[0], ((t0[1] + t1[1]) + t2[1]) + t3[1]);
                unsigned w1 = pk2(((t0[2] + t1[2]) + t2[2]) + t3[2], ((t0[3] + t1[3]) + t2[3]) + t3[3]);
                if (w0 == POISON) w0 = 0x7FC07FC0u;
                if (w1 == POISON) w1 = 0x7FC07FC0u;
                st8_sc1(MB + ((size_t)(s % 3) * CH_WG + b) * 32 + m * 4 + h2 * 2, (u32x2){w0, w1});
            } else if (s >= 1) {
                st8_sc1(MB + ((size_t)((s + 2) % 3) * CH_WG + b) * 32 + m * 4 + h2 * 2, (u32x2){POISON, POISON});
            }
        }
        {
            const u32x4 pz = {POISON, POISON, POISON, POISON};
#pragma unroll
            for (int j = 0; j < 4; ++j) st16_sc1((unsigned*)gb + (size_t)j * 16 * 64, pz);
        }
        if (dead) break;
        {
            const unsigned* mbb = MB + (size_t)(s % 3) * CH_WG * 32 + tid * 4;
            sleep_units(CH_D0);
            unsigned spins = 0;
            for (;;) {
                u32x4 v[2];
                ld16_sc1(v[0], mbb); ld16_sc1(v[1], mbb + 1024);
                WAITV();
                asm volatile("" : "+v"(v[0])); asm volatile("" : "+v"(v[1]));
                if (__all(okv(v[0]) & okv(v[1]))) {
                    const int i0 = tid, i1 = tid + 256;
                    memf[((i0 >> 3) >> 2) * 64 + ((i0 >> 3) & 3) * 16 + (i0 & 7)] = v[0];
                    memf[((i1 >> 3) >> 2) * 64 + ((i1 >> 3) & 3) * 16 + (i1 & 7)] = v[1];
                    break;
                }
                if (++spins > (1u << 18)) { __hip_atomic_store((__attribute__((address_space(1))) unsigned*)abortw, 1u, __ATOMIC_RELAXED, __HIP_MEMORY_SCOPE_AGENT); dead = true; break; }
                if ((spins & 63) == 0 && __hip_atomic_load((__attribute__((address_space(1))) unsigned*)abortw, __ATOMIC_RELAXED, __HIP_MEMORY_SCOPE_AGENT)) { dead = true; break; }
            }
        }
        __syncthreads();
        if (dead) break;
    }
}

__global__ __launch_bounds__(256) void tokattn_kernel(const u16* __restrict__ QK, const u16* __restrict__ VT, const u32x4* __restrict__ KM,
                                                       const u32x2* __restrict__ VM, u16* __restrict__ o) {
    const int tid = threadIdx.x, w = tid >> 6, lane = tid & 63, g = lane >> 4, r16 = lane & 15;
    const int pair = blockIdx.x * 4 + w, s = pair >> 5, h = pair & 31;
    const float cs = 0.08838834764831845f * 1.4426950408889634f;
    const int keyr = 8 * (r16 >> 2) + (r16 & 3);
    const u16* qp = QK + (size_t)(s * SEG + r16) * 8192 + h * DHB + 8 * g;
    const u16* kp = QK + (size_t)(s * SEG + keyr) * 8192 + NB + h * DHB + 8 * g;
    const f32x4 z4 = {0.f, 0.f, 0.f, 0.f};
    f32x4 St[2][3];
#pragma unroll
    for (int qt = 0; qt < 2; ++qt)
#pragma unroll
        for (int kt = 0; kt < 3; ++kt) St[qt][kt] = z4;
#pragma unroll
    for (int ks = 0; ks < 4; ++ks) {
        const bf16x8 q0 = __builtin_bit_cast(bf16x8, *(const u32x4*)(qp + 32 * ks));
        const bf16x8 q1 = __builtin_bit_cast(bf16x8, *(const u32x4*)(qp + (size_t)16 * 8192 + 32 * ks));
        const bf16x8 k0 = __builtin_bit_cast(bf16x8, *(const u32x4*)(kp + 32 * ks));
        const bf16x8 k1 = __builtin_bit_cast(bf16x8, *(const u32x4*)(kp + (size_t)4 * 8192 + 32 * ks));
        const bf16x8 km = __builtin_bit_cast(bf16x8, KM[((size_t)(s * HB + h) * 4 + ks) * 64 + lane]);
        St[0][0] = __builtin_amdgcn_mfma_f32_16x16x32_bf16(k0, q0, St[0][0], 0, 0, 0);
        St[0][1] = __builtin_amdgcn_mfma_f32_16x16x32_bf16(k1, q0, St[0][1], 0, 0, 0);
        St[0][2] = __builtin_amdgcn_mfma_f32_16x16x32_bf16(km, q0, St[0][2], 0, 0, 0);
        St[1][0] = __builtin_amdgcn_mfma_f32_16x16x32_bf16(k0, q1, St[1][0], 0, 0, 0);
        St[1][1] = __builtin_amdgcn_mfma_f32_16x16x32_bf16(k1, q1, St[1][1], 0, 0, 0);
        St[1][2] = __builtin_amdgcn_mfma_f32_16x16x32_bf16(km, q1, St[1][2], 0, 0, 0);
    }
    bf16x8 pf[2][2];
#pragma unroll
    for (int qt = 0; qt < 2; ++qt) {
        const int qi = 16 * qt + r16;
        float sc[12];
#pragma unroll
        for (int i = 0; i < 4; ++i) {
            sc[i] = (8 * g + i <= qi) ? St[qt][0][i] : -INFINITY;
            sc[4 + i] = (8 * g + 4 + i <= qi) ? St[qt][1][i] : -INFINITY;
            sc[8 + i] = (g < 2) ? St[qt][2][i] : -INFINITY;
        }
        float mx = sc[0];
#pragma unroll
        for (int i = 1; i < 12; ++i) mx = fmaxf(mx, sc[i]);
        mx = xmax16_32(mx);
        const float mc = mx * cs;
        float sum = 0.f;
#pragma unroll
        for (int i = 0; i < 12; ++i) { sc[i] = exp2f(__builtin_fmaf(sc[i], cs, -mc)); sum += sc[i]; }
        sum = xsum16_32(sum);
        const float inv = 1.0f / sum;
#pragma unroll
        for (int i = 0; i < 4; ++i) { pf[qt][0][i] = (__bf16)(sc[i] * inv); pf[qt][0][4 + i] = (__bf16)(sc[4 + i] * inv); pf[qt][1][i] = (__bf16)(sc[8 + i] * inv); pf[qt][1][4 + i] = (__bf16)0.f; }
    }
#pragma unroll
    for (int a = 0; a < 2; ++a)
#pragma unroll
        for (int wv = 0; wv < 4; ++wv) {
            const int dl = 32 * (wv >> 1) + 8 * (r16 >> 2) + 4 * (wv & 1) + (r16 & 3);
            const bf16x8 vtf = __builtin_bit_cast(bf16x8, *(const u32x4*)(VT + (size_t)(h * DHB + 64 * a + dl) * S_LEN + s * SEG + 8 * g));
            const u32x2 vm2 = VM[(((size_t)(s * HB + h) * 2 + a) * 4 + wv) * 64 + lane];
            const bf16x8 vmf = __builtin_bit_cast(bf16x8, (u32x4){vm2.x, vm2.y, 0u, 0u});
#pragma unroll
            for (int qt = 0; qt < 2; ++qt) {
                f32x4 ao = __builtin_amdgcn_mfma_f32_16x16x32_bf16(vtf, pf[qt][0], z4, 0, 0, 0);
                ao = __builtin_amdgcn_mfma_f32_16x16x32_bf16(vmf, pf[qt][1], ao, 0, 0, 0);
                u16* op = o + (size_t)(s * SEG + 16 * qt + r16) * NB + h * DHB + 64 * a + 32 * (wv >> 1) + 8 * g + 4 * (wv & 1);
                *(u32x2*)op = (u32x2){pk2(ao[0], ao[1]), pk2(ao[2], ao[3])};
            }
        }
}

}

extern "C" void kernel_launch(void* const* d_in, const int* in_sizes, int n_in,
                              void* d_out, int out_size, void* d_ws, size_t ws_size,
                              hipStream_t stream) {
    const u16* A_x = (const u16*)d_in[0];
    const u16* A_wq = (const u16*)d_in[1];
    const u16* A_wk = (const u16*)d_in[2];
    const u16* A_wv = (const u16*)d_in[3];
    const u16* A_wo = (const u16*)d_in[4];
    const u16* B_mem0 = (const u16*)d_in[6];
    const u16* B_Wq = (const u16*)d_in[7];
    const u16* B_Wk = (const u16*)d_in[8];
    const u16* B_Wv = (const u16*)d_in[9];
    const u16* B_Wo = (const u16*)d_in[10];
    u16* out = (u16*)d_out;

    unsigned char* ws = (unsigned char*)d_ws;
    size_t off = 0;
    auto take = [&](size_t bytes) { unsigned char* p = ws + off; off += (bytes + 255) & ~(size_t)255; return p; };
    u16* wtB = (u16*)take((size_t)3 * NB * DM * 2);
    u16* wtoB = (u16*)take((size_t)DM * NB * 2);
    u16* wtA = (u16*)take((size_t)QKVA_LD * DM * 2);
    u16* wtoA = (u16*)take((size_t)DM * DM * 2);
    u16* xa = (u16*)take((size_t)S_LEN * DM * 2);
    u16* xb = (u16*)take((size_t)S_LEN * DM * 2);
    u16* xn = (u16*)take((size_t)S_LEN * DM * 2);
    u16* qkvA = (u16*)take((size_t)S_LEN * QKVA_LD * 2);
    u16* oA = (u16*)take((size_t)S_LEN * DM * 2);
    u16* QK = (u16*)take((size_t)S_LEN * 8192 * 2);
    u16* VT = (u16*)take((size_t)NB * S_LEN * 2);
    u16* oB = (u16*)take((size_t)S_LEN * NB * 2);
    u32x4* KM = (u32x4*)take((size_t)NSEG * HB * 4 * 64 * 16);
    u32x2* VM = (u32x2*)take((size_t)NSEG * HB * 2 * 4 * 64 * 8);
    unsigned* xchg = (unsigned*)take((size_t)(PB_WORDS + MB_WORDS + 64) * 4);
    unsigned* PB = xchg;
    unsigned* MB = xchg + PB_WORDS;
    unsigned* abortw = (unsigned*)take(256);
    const int xchg_vec = (PB_WORDS + MB_WORDS) / 4;

    hipMemsetAsync(abortw, 0, 256, stream);
    prep_kernel<<<2193, 256, 0, stream>>>(B_Wq, B_Wk, B_Wv, B_Wo, A_wq, A_wk, A_wv, A_wo, wtB, wtoB, wtA, wtoA);

    const int attnA_lds = 65536 + 16 * AVROW + 128;
    hipFuncSetAttribute((const void*)attnA_kernel, hipFuncAttributeMaxDynamicSharedMemorySize, attnA_lds);

    const u16* xin = A_x;
    u16* xouts[4] = {xa, xb, xa, out};
    for (int layer = 0; layer < 4; ++layer) {
        u16* xo = xouts[layer];
        if ((layer & 1) == 0) {
            ln_kernel<<<S_LEN / 4, 256, 0, stream>>>(xin, xn, nullptr, 0);
            gemm_nt<128, 128, 0><<<(S_LEN / 128) * (QKVA_LD / 128), 256, 0, stream>>>(xn, DM, wtA, DM, qkvA, QKVA_LD, nullptr, 0, DM, QKVA_LD / 128);
            attnA_kernel<<<S_LEN / 8, 512, attnA_lds, stream>>>(qkvA, oA);
            gemm_nt<64, 64, 1><<<(S_LEN / 64) * (DM / 64), 256, 0, stream>>>(oA, DM, wtoA, DM, xo, DM, xin, DM, DM, DM / 64);
        } else {
            ln_kernel<<<S_LEN / 4, 256, 0, stream>>>(xin, xn, (u32x4*)xchg, xchg_vec);
            gemm_nt<128, 128, 0><<<(S_LEN / 128) * (8192 / 128), 256, 0, stream>>>(xn, DM, wtB, DM, QK, 8192, nullptr, 0, DM, 8192 / 128);
            gemm_nt<128, 128, 0><<<(NB / 128) * (S_LEN / 128), 256, 0, stream>>>(wtB + (size_t)2 * NB * DM, DM, xn, DM, VT, S_LEN, nullptr, 0, DM, S_LEN / 128);
            chain_kernel<<<CH_WG, 256, 0, stream>>>(wtB, wtoB, QK, VT, B_mem0, KM, VM, PB, MB, abortw);
            tokattn_kernel<<<NSEG * HB / 4, 256, 0, stream>>>(QK, VT, KM, VM, oB);
            gemm_nt<64, 64, 1><<<(S_LEN / 64) * (DM / 64), 256, 0, stream>>>(oB, NB, wtoB, NB, xo, DM, xin, DM, NB, DM / 64);
        }
        xin = xo;
    }
}
```

```cpp
#include <hip/hip_runtime.h>
#include <hip/hip_bf16.h>
#include <stdint.h>

typedef unsigned short u16;
typedef __attribute__((ext_vector_type(8))) __bf16 bf16x8;
typedef __attribute__((ext_vector_type(4))) __bf16 bf16x4;
typedef __attribute__((ext_vector_type(4))) float f32x4;
typedef __attribute__((ext_vector_type(16))) float f32x16;
typedef __attribute__((ext_vector_type(4))) unsigned u32x4;
typedef __attribute__((ext_vector_type(2))) unsigned u32x2;

namespace {
#define WAITV() asm volatile("s_waitcnt vmcnt(0)" ::: "memory")
#define LDS_BARRIER() asm volatile("s_waitcnt lgkmcnt(0)\n\ts_barrier" ::: "memory")

constexpr int S_LEN = 2048, DM = 512;
constexpr int NSEG = 64, SEG = 32, MEMR = 8;
constexpr int HB = 32, DHB = 128, NB = HB * DHB;
constexpr int QKVA_LD = 640;

__device__ __forceinline__ float bf2f(u16 v) { return __uint_as_float(((unsigned)v) << 16); }
__device__ __forceinline__ u16 f2bf(float f) { __bf16 b = (__bf16)f; return __builtin_bit_cast(u16, b); }
__device__ __forceinline__ float rbf(float f) { return bf2f(f2bf(f)); }
__device__ __forceinline__ unsigned pk2(float a, float b) { return (unsigned)f2bf(a) | ((unsigned)f2bf(b) << 16); }

__device__ __forceinline__ float xsum16_32(float x) {
    auto a = __builtin_amdgcn_permlane16_swap(__float_as_uint(x), __float_as_uint(x), false, false);
    x = __uint_as_float(a[0]) + __uint_as_float(a[1]);
    auto b = __builtin_amdgcn_permlane32_swap(__float_as_uint(x), __float_as_uint(x), false, false);
    return __uint_as_float(b[0]) + __uint_as_float(b[1]);
}
__device__ __forceinline__ float xmax16_32(float x) {
    auto a = __builtin_amdgcn_permlane16_swap(__float_as_uint(x), __float_as_uint(x), false, false);
    x = fmaxf(__uint_as_float(a[0]), __uint_as_float(a[1]));
    auto b = __builtin_amdgcn_permlane32_swap(__float_as_uint(x), __float_as_uint(x), false, false);
    return fmaxf(__uint_as_float(b[0]), __uint_as_float(b[1]));
}

struct TJob { const u16* src; u16* dst; int R; int C; int ldd; int tile0; };

__global__ __launch_bounds__(256) void prep_kernel(const u16* bq, const u16* bk, const u16* bv, const u16* bo,
                                                    const u16* aq, const u16* ak, const u16* av, const u16* ao,
                                                    u16* wtB, u16* wtoB, u16* wtA, u16* wtoA) {
    __shared__ u16 tile[64][72];
    int t = blockIdx.x;
    const u16* src; u16* dst; int R, C, ldd;
    if (t < 1536)      { int j = t >> 9; t &= 511; src = j == 0 ? bq : (j == 1 ? bk : bv); dst = wtB + (size_t)j * 4096 * 512; R = 512; C = 4096; ldd = 512; }
    else if (t < 2048) { t -= 1536; src = bo; dst = wtoB; R = 4096; C = 512; ldd = 4096; }
    else if (t < 2112) { t -= 2048; src = aq; dst = wtA; R = 512; C = 512; ldd = 512; }
    else if (t < 2176) { t -= 2112; src = ao; dst = wtoA; R = 512; C = 512; ldd = 512; }
    else if (t < 2184) { t -= 2176; src = ak; dst = wtA + 512 * 512; R = 512; C = 16; ldd = 512; }
    else if (t < 2192) { t -= 2184; src = av; dst = wtA + 528 * 512; R = 512; C = 16; ldd = 512; }
    else {
        u32x4 z = {0u, 0u, 0u, 0u};
        u32x4* p = (u32x4*)(wtA + 544 * 512);
        for (int i = threadIdx.x; i < 96 * 512 / 8; i += 256) p[i] = z;
        return;
    }
    const int tc = (C + 63) >> 6;
    const int r0 = (t / tc) * 64, c0 = (t % tc) * 64;
    const int tid = threadIdx.x;
    for (int i = 0; i < 2; ++i) {
        const int r = (tid >> 3) + 32 * i, ch = tid & 7;
        const int c = c0 + ch * 8;
        if (c < C) {
            u32x4 v = *(const u32x4*)(src + (size_t)(r0 + r) * C + c);
            u16* d = &tile[r][ch * 8];
            d[0] = (u16)v.x; d[1] = (u16)(v.x >> 16); d[2] = (u16)v.y; d[3] = (u16)(v.y >> 16);
            d[4] = (u16)v.z; d[5] = (u16)(v.z >> 16); d[6] = (u16)v.w; d[7] = (u16)(v.w >> 16);
        }
    }
    __syncthreads();
    for (int i = 0; i < 2; ++i) {
        const int c = (tid >> 3) + 32 * i, ch = tid & 7;
        if (c0 + c < C) {
            u32x4 v;
            v.x = (unsigned)tile[ch * 8 + 0][c] | ((unsigned)tile[ch * 8 + 1][c] << 16);
            v.y = (unsigned)tile[ch * 8 + 2][c] | ((unsigned)tile[ch * 8 + 3][c] << 16);
            v.z = (unsigned)tile[ch * 8 + 4][c] | ((unsigned)tile[ch * 8 + 5][c] << 16);
            v.w = (unsigned)tile[ch * 8 + 6][c] | ((unsigned)tile[ch * 8 + 7][c] << 16);
            *(u32x4*)(dst + (size_t)(c0 + c) * ldd + r0 + ch * 8) = v;
        }
    }
}

__global__ __launch_bounds__(256) void ln_kernel(const u16* __restrict__ x, u16* __restrict__ xn, u32x4* xchg, int xchg_vec) {
    const int tid = threadIdx.x, w = tid >> 6, lane = tid & 63;
    const int row = blockIdx.x * 4 + w;
    if (xchg != nullptr) {
        const u32x4 pz = {0xFFFFFFFFu, 0xFFFFFFFFu, 0xFFFFFFFFu, 0xFFFFFFFFu};
        for (int i = blockIdx.x * 256 + tid; i < xchg_vec; i += gridDim.x * 256) xchg[i] = pz;
    }
    u32x4 v = *(const u32x4*)(x + (size_t)row * DM + lane * 8);
    float f[8];
    f[0] = bf2f((u16)v.x); f[1] = bf2f((u16)(v.x >> 16)); f[2] = bf2f((u16)v.y); f[3] = bf2f((u16)(v.y >> 16));
    f[4] = bf2f((u16)v.z); f[5] = bf2f((u16)(v.z >> 16)); f[6] = bf2f((u16)v.w); f[7] = bf2f((u16)(v.w >> 16));
    float s = 0.f;
#pragma unroll
    for (int i = 0; i < 8; ++i) s += f[i];
#pragma unroll
    for (int o = 32; o >= 1; o >>= 1) s += __shfl_xor(s, o);
    const float mean = s * (1.0f / DM);
    float q = 0.f;
#pragma unroll
    for (int i = 0; i < 8; ++i) { const float d = f[i] - mean; q += d * d; }
#pragma unroll
    for (int o = 32; o >= 1; o >>= 1) q += __shfl_xor(q, o);
    const float var = q * (1.0f / DM);
    const float mb = rbf(mean);
    const float den = rbf(sqrtf(rbf(rbf(var) + 1e-5f)));
    u32x4 r;
    float y[8];
#pragma unroll
    for (int i = 0; i < 8; ++i) y[i] = rbf(f[i] - mb) / den;
    r.x = pk2(y[0], y[1]); r.y = pk2(y[2], y[3]); r.z = pk2(y[4], y[5]); r.w = pk2(y[6], y[7]);
    *(u32x4*)(xn + (size_t)row * DM + lane * 8) = r;
}

template <int BM, int BN, int EPI, int SC1>
__device__ __forceinline__ void gemm_tile(u32x4* smem, const u16* __restrict__ A, int lda, const u16* __restrict__ Bt, int ldb,
                                          u16* __restrict__ C, int ldc, const u16* __restrict__ R, int ldr, int K, int bm, int bn) {
    constexpr int MT = BM / 32, NT = BN / 32;
    constexpr int LA = BM * 8 / 256, LB = BN * 8 / 256;
    u32x4* const sA = smem;
    u32x4* const sB = smem + 2 * BM * 8;
    const int tid = threadIdx.x, w = tid >> 6, lane = tid & 63, g = lane >> 4, r16 = lane & 15;
    const int wm = w >> 1, wn = w & 1;
    f32x4 acc[NT][MT];
#pragma unroll
    for (int i = 0; i < NT; ++i)
#pragma unroll
        for (int j = 0; j < MT; ++j) acc[i][j] = (f32x4){0.f, 0.f, 0.f, 0.f};
    u32x4 ra[2][LA], rb[2][LB];
    const u16* Ap = A + (size_t)(bm + (tid >> 3)) * lda + (tid & 7) * 8;
    const u16* Bp = Bt + (size_t)(bn + (tid >> 3)) * ldb + (tid & 7) * 8;
    const int srow = tid >> 3, sc = tid & 7;
    auto gload = [&](int set, int k0) {
#pragma unroll
        for (int i = 0; i < LA; ++i) ra[set][i] = *(const u32x4*)(Ap + (size_t)(32 * i) * lda + k0);
#pragma unroll
        for (int i = 0; i < LB; ++i) rb[set][i] = *(const u32x4*)(Bp + (size_t)(32 * i) * ldb + k0);
    };
    auto sstore = [&](int set, int buf) {
#pragma unroll
        for (int i = 0; i < LA; ++i) { const int row = srow + 32 * i; sA[buf * BM * 8 + row * 8 + (sc ^ ((row >> 1) & 7))] = ra[set][i]; }
#pragma unroll
        for (int i = 0; i < LB; ++i) { const int row = srow + 32 * i; sB[buf * BN * 8 + row * 8 + (sc ^ ((row >> 1) & 7))] = rb[set][i]; }
    };
    auto compute = [&](int buf) {
#pragma unroll
        for (int ks = 0; ks < 2; ++ks) {
            bf16x8 fa[MT], fb[NT];
#pragma unroll
            for (int j = 0; j < MT; ++j) { const int row = wm * (BM / 2) + j * 16 + r16, c = ks * 4 + g; fa[j] = __builtin_bit_cast(bf16x8, sA[buf * BM * 8 + row * 8 + (c ^ ((row >> 1) & 7))]); }
#pragma unroll
            for (int i = 0; i < NT; ++i) { const int row = wn * (BN / 2) + i * 16 + r16, c = ks * 4 + g; fb[i] = __builtin_bit_cast(bf16x8, sB[buf * BN * 8 + row * 8 + (c ^ ((row >> 1) & 7))]); }
#pragma unroll
            for (int i = 0; i < NT; ++i)
#pragma unroll
                for (int j = 0; j < MT; ++j) acc[i][j] = __builtin_amdgcn_mfma_f32_16x16x32_bf16(fb[i], fa[j], acc[i][j], 0, 0, 0);
        }
    };
    const int nst = K >> 6;
    gload(0, 0);
    gload(1, 64);
    sstore(0, 0);
    LDS_BARRIER();
    for (int st = 0; st < nst; st += 2) {
        if (st + 2 < nst) gload(0, (st + 2) << 6);
        compute(0);
        sstore(1, 1);
        LDS_BARRIER();
        if (st + 3 < nst) gload(1, (st + 3) << 6);
        compute(1);
        if (st + 2 < nst) sstore(0, 0);
        LDS_BARRIER();
    }
    constexpr int TLD = BN + 8;
    u16* const tile = (u16*)smem;
#pragma unroll
    for (int i = 0; i < NT; ++i)
#pragma unroll
        for (int j = 0; j < MT; ++j) {
            const int m = wm * (BM / 2) + j * 16 + r16, n = wn * (BN / 2) + i * 16 + 4 * g;
            const f32x4 v = acc[i][j];
            *(u32x2*)(tile + m * TLD + n) = (u32x2){pk2(v[0], v[1]), pk2(v[2], v[3])};
        }
    LDS_BARRIER();
    constexpr int CPR = BN / 8;
#pragma unroll
    for (int i = 0; i < BM * CPR / 256; ++i) {
        const int idx = tid + 256 * i, m = idx / CPR, c = idx % CPR;
        u32x4 v = *(const u32x4*)(tile + m * TLD + c * 8);
        if (EPI == 1) {
            const u32x4 rr = *(const u32x4*)(R + (size_t)(bm + m) * ldr + bn + c * 8);
            const unsigned vv[4] = {v.x, v.y, v.z, v.w}, r4[4] = {rr.x, rr.y, rr.z, rr.w};
            unsigned o4[4];
#pragma unroll
            for (int e = 0; e < 4; ++e) o4[e] = pk2(bf2f((u16)r4[e]) + bf2f((u16)vv[e]), bf2f((u16)(r4[e] >> 16)) + bf2f((u16)(vv[e] >> 16)));
            v = (u32x4){o4[0], o4[1], o4[2], o4[3]};
        }
        u16* cp = C + (size_t)(bm + m) * ldc + bn + c * 8;
        if (SC1) asm volatile("global_store_dwordx4 %0, %1, off sc1\n\ts_nop 1" :: "v"(cp), "v"(v) : "memory");
        else *(u32x4*)cp = v;
    }
    LDS_BARRIER();
}

template <int BM, int BN, int EPI>
__global__ __launch_bounds__(256, 2) void gemm_nt(const u16* __restrict__ A, int lda, const u16* __restrict__ Bt, int ldb,
                                                   u16* __restrict__ C, int ldc, const u16* __restrict__ R, int ldr, int K, int ntn) {
    __shared__ u32x4 smem[2 * (BM + BN) * 8];
    const int bid = blockIdx.x;
    const int bn = (bid % ntn) * BN, bm = (bid / ntn) * BM;
    gemm_tile<BM, BN, EPI, 0>(smem, A, lda, Bt, ldb, C, ldc, R, ldr, K, bm, bn);
}

namespace g256 {
constexpr int BM = 256, BK = 64, HALF = 128, HT = HALF * BK, SHM_B = 8 * HT * 2;
__device__ __forceinline__ int lds_byte(int r, int c) {
    int st = (r >> 4) * 2 + (c >> 5), rr = r & 15, cc = c & 31, ob = rr * 64 + cc * 2;
    return st * 1024 + (ob ^ (((ob >> 9) & 1) << 5));
}
__device__ __forceinline__ void stage_rc(int b, int& R, int& C) {
    int st = b / 1024, sb = b % 1024, swz = sb ^ (((sb >> 9) & 1) << 5);
    R = (st >> 1) * 16 + swz / 64; C = (st & 1) * 32 + (swz % 64) / 2;
}
}
__global__ __launch_bounds__(512, 2) void gemm256_kernel(const u16* __restrict__ A, const u16* __restrict__ Bt, u16* __restrict__ C,
                                                          int M, int N, int K, int ldc) {
    extern __shared__ __attribute__((aligned(16))) u16 shm[];
    using namespace g256;
    typedef __attribute__((ext_vector_type(8))) short bf16s8;
    #define SA(b,h) (shm+((b)*2+(h))*HT)
    #define SB(b,h) (shm+(4+(b)*2+(h))*HT)
    #define STAGE_X(T,P,BASE,br,kt) do{long _g=(long)(br)*K+(long)(kt)*BK; \
      for(int _i=0;_i<2;++_i){int _b=(T)*16+_i*8192;int _r,_c;stage_rc(_b,_r,_c); \
        __builtin_amdgcn_global_load_lds((const unsigned*)(BASE+_g+(long)_r*K+_c), \
          (unsigned*)((char*)(P)+_b),16,0,0);}}while(0)
    #define STAGE(P,BASE,br,kt) STAGE_X(threadIdx.x,P,BASE,br,kt)
    #define LDA(dst,b,h) for(int m=0;m<4;++m)for(int k=0;k<2;++k) \
      dst[m][k]=*reinterpret_cast<const bf16s8*>((char*)SA(b,h)+lds_byte(wr*64+m*16+fr,k*32+fq*8))
    #define LDB(dst,b,h) for(int n=0;n<2;++n)for(int k=0;k<2;++k) \
      dst[n][k]=*reinterpret_cast<const bf16s8*>((char*)SB(b,h)+lds_byte(wc*32+n*16+fr,k*32+fq*8))
    #define MMA(ai,bj,At,Bt) do{__builtin_amdgcn_s_setprio(1); \
      for(int m=0;m<4;++m)for(int n=0;n<2;++n)for(int k=0;k<2;++k) \
        acc[ai][bj][m][n]=__builtin_amdgcn_mfma_f32_16x16x32_bf16(At[m][k],Bt[n][k],acc[ai][bj][m][n],0,0,0); \
      __builtin_amdgcn_s_setprio(0);}while(0)
    #define WAIT_V(n) asm volatile("s_waitcnt vmcnt(" #n ")":::"memory")
    #define WAIT_L(n) asm volatile("s_waitcnt lgkmcnt(" #n ")":::"memory")
    #define BAR __builtin_amdgcn_s_barrier()
    #define SCHED __builtin_amdgcn_sched_barrier(0)
    int nM = M / BM, nN = N / BM, nwg = nM * nN, wgid = blockIdx.x;
    {int q = nwg / 8, r = nwg % 8, xcd = wgid % 8, off = wgid / 8;
     wgid = (xcd < r ? xcd * (q + 1) : r * (q + 1) + (xcd - r) * q) + off;}
    int nig = 8 * nN, gid = wgid / nig, fm = gid * 8, gsz = min(nM - fm, 8);
    int pm = fm + ((wgid % nig) % gsz), pn = (wgid % nig) / gsz, brow = pm * BM, bcol = pn * BM;
    int wid = threadIdx.x >> 6, lane = threadIdx.x & 63, wr = wid >> 2, wc = wid & 3, fr = lane & 15, fq = lane >> 4;
    f32x4 acc[2][2][4][2] = {};
    bf16s8 At[4][2], B0[2][2], B1[2][2];
    int nt = K / BK;
    STAGE(SB(0,0),Bt,bcol,0); STAGE(SA(0,0),A,brow,0);
    STAGE(SB(0,1),Bt,bcol+HALF,0); STAGE(SA(0,1),A,brow+HALF,0);
    if(wr==1)BAR;
    WAIT_V(4); BAR;
    STAGE(SB(1,0),Bt,bcol,1); STAGE(SA(1,0),A,brow,1); STAGE(SB(1,1),Bt,bcol+HALF,1);
    WAIT_V(6); BAR;
    for(int t=0;t<nt-2;t+=2){
      LDB(B0,0,0); SCHED; LDA(At,0,0); STAGE(SA(1,1),A,brow+HALF,t+1);
      WAIT_L(8); BAR; WAIT_L(0); MMA(0,0,At,B0); BAR; SCHED;
      LDB(B1,0,1); STAGE(SB(0,0),Bt,bcol,t+2);
      BAR; WAIT_L(0); MMA(0,1,At,B1); BAR;
      LDA(At,0,1); STAGE(SA(0,0),A,brow,t+2);
      BAR; WAIT_L(0); MMA(1,0,At,B0); BAR; SCHED;
      STAGE(SB(0,1),Bt,bcol+HALF,t+2);
      WAIT_V(6); BAR; MMA(1,1,At,B1); BAR;
      LDB(B0,1,0); SCHED; LDA(At,1,0); STAGE(SA(0,1),A,brow+HALF,t+2);
      WAIT_L(8); BAR; WAIT_L(0); MMA(0,0,At,B0); BAR; SCHED;
      LDB(B1,1,1); STAGE(SB(1,0),Bt,bcol,t+3);
      BAR; WAIT_L(0); MMA(0,1,At,B1); BAR;
      LDA(At,1,1); STAGE(SA(1,0),A,brow,t+3);
      BAR; WAIT_L(0); MMA(1,0,At,B0); BAR; SCHED;
      STAGE(SB(1,1),Bt,bcol+HALF,t+3);
      WAIT_V(6); BAR; MMA(1,1,At,B1); BAR;
    }
    int tz=threadIdx.x; asm volatile("":"+v"(tz)); wid=tz>>6; lane=tz&63; wr=wid>>2; wc=wid&3; fr=lane&15; fq=lane>>4;
    { LDB(B0,0,0); WAIT_V(0); LDA(At,0,0); STAGE_X(tz,SA(1,1),A,brow+HALF,nt-1);
      BAR; WAIT_L(0); MMA(0,0,At,B0); BAR;
      LDB(B1,0,1); BAR; WAIT_L(0); MMA(0,1,At,B1); BAR;
      LDA(At,0,1); WAIT_V(4); BAR; WAIT_L(0); MMA(1,0,At,B0); MMA(1,1,At,B1); BAR; }
    { LDB(B0,1,0); LDA(At,1,0); WAIT_V(2); BAR; WAIT_L(0); MMA(0,0,At,B0); BAR;
      LDB(B1,1,1); WAIT_V(0); BAR; WAIT_L(0); MMA(0,1,At,B1); BAR;
      LDA(At,1,1); BAR; WAIT_L(0); MMA(1,0,At,B0); MMA(1,1,At,B1); BAR; }
    if(wr==0)BAR;
    constexpr int TLD = BM;
    u16* tile = shm;
    BAR;
    for(int ai=0;ai<2;++ai)for(int bj=0;bj<2;++bj)for(int m=0;m<4;++m)for(int n=0;n<2;++n) {
      const f32x4 v = acc[ai][bj][m][n];
      const int rl = ai*HALF + wr*64 + m*16 + fq*4, cl = bj*HALF + wc*32 + n*16 + fr;
      tile[(rl + 0) * TLD + cl] = (u16)(f2bf(v[0]));
      tile[(rl + 1) * TLD + cl] = f2bf(v[1]);
      tile[(rl + 2) * TLD + cl] = f2bf(v[2]);
      tile[(rl + 3) * TLD + cl] = f2bf(v[3]);
    }
    BAR;
    for (int idx = threadIdx.x; idx < BM * (BM / 8); idx += 512) {
      const int rl = idx / (BM / 8), chl = idx % (BM / 8);
      u32x4 v = *(const u32x4*)(tile + rl * TLD + chl * 8);
      *(u32x4*)(C + (size_t)(brow + rl) * ldc + bcol + chl * 8) = v;
    }
}
#undef SA
#undef SB
#undef STAGE_X
#undef STAGE
#undef LDA
#undef LDB
#undef MMA
#undef WAIT_V
#undef WAIT_L
#undef BAR
#undef SCHED

constexpr int AVROW = S_LEN * 2 + 16;
__global__ __launch_bounds__(1024, 1) void attnA_kernel(const u16* __restrict__ qkv, u16* __restrict__ o) {
    extern __shared__ __attribute__((aligned(16))) unsigned char smem[];
    unsigned char* sK = smem;
    unsigned char* sV = smem + 65536;
    unsigned char* sC = sV + 16 * AVROW;
    const int tid = threadIdx.x, w = tid >> 6, lane = tid & 63;
    for (int i = tid; i < S_LEN * 2; i += 1024) {
        const int key = i >> 1, hf = i & 1;
        *(u32x4*)(sK + key * 32 + hf * 16) = *(const u32x4*)(qkv + (size_t)key * QKVA_LD + 512 + hf * 8);
    }
    if (tid < S_LEN / 4) {
        const int key4 = tid * 4;
        u32x4 v[4][2];
#pragma unroll
        for (int k = 0; k < 4; ++k) {
            v[k][0] = *(const u32x4*)(qkv + (size_t)(key4 + k) * QKVA_LD + 528);
            v[k][1] = *(const u32x4*)(qkv + (size_t)(key4 + k) * QKVA_LD + 536);
        }
        const int kk = key4 & 31;
        const int pos = (key4 & ~31) + (kk & 16) + ((kk >> 2) & 1) * 8 + ((kk >> 3) & 1) * 4;
#pragma unroll
        for (int d2 = 0; d2 < 8; ++d2) {
            const unsigned a0 = v[0][d2 >> 2][d2 & 3], a1 = v[1][d2 >> 2][d2 & 3], a2 = v[2][d2 >> 2][d2 & 3], a3 = v[3][d2 >> 2][d2 & 3];
            const u32x2 lo = {(a0 & 0xFFFFu) | (a1 << 16), (a2 & 0xFFFFu) | (a3 << 16)};
            const u32x2 hi = {(a0 >> 16) | (a1 & 0xFFFF0000u), (a2 >> 16) | (a3 & 0xFFFF0000u)};
            *(u32x2*)(sV + (2 * d2) * AVROW + pos * 2) = lo;
            *(u32x2*)(sV + (2 * d2 + 1) * AVROW + pos * 2) = hi;
        }
    }
    if (tid < 32) ((u16*)sC)[tid] = 0x3F80;
    else if (tid < 64) ((u16*)sC)[tid] = 0;
    __syncthreads();

    const int token = blockIdx.x * 8 + (w & 7), khalf = w >> 3;
    const int hh = lane >> 5, l32 = lane & 31;
    const bf16x8 qf = __builtin_bit_cast(bf16x8, *(const u32x4*)(qkv + (size_t)token * QKVA_LD + l32 * 16 + hh * 8));
    f32x16 O;
#pragma unroll
    for (int i = 0; i < 16; ++i) O[i] = 0.f;
    float mrun = -INFINITY;
    const float cs = 0.25f * 1.4426950408889634f;
    const int row = l32;
    const unsigned char* vbase = (row < 16) ? (sV + row * AVROW + hh * 16) : (sC + (row == 16 ? 0 : 64));
    const int vstep = (row < 16) ? 64 : 0;
    const int vhalf = (row < 16) ? 32 : 0;
    f32x16 Z;
#pragma unroll
    for (int i = 0; i < 16; ++i) Z[i] = 0.f;
    constexpr int NTH = S_LEN / 64;
    const int t0 = khalf * NTH;
    const unsigned char* kptr = sK + l32 * 32 + hh * 16;
    f32x16 Sn = __builtin_amdgcn_mfma_f32_32x32x16_bf16(__builtin_bit_cast(bf16x8, *(const u32x4*)(kptr + t0 * 1024)), qf, Z, 0, 0, 0);
    for (int t = t0; t < t0 + NTH; ++t) {
        const f32x16 Sx = Sn;
        const bf16x8 vf0 = __builtin_bit_cast(bf16x8, *(const u32x4*)(vbase + t * vstep));
        const bf16x8 vf1 = __builtin_bit_cast(bf16x8, *(const u32x4*)(vbase + t * vstep + vhalf));
        if (t + 1 < t0 + NTH) Sn = __builtin_amdgcn_mfma_f32_32x32x16_bf16(__builtin_bit_cast(bf16x8, *(const u32x4*)(kptr + (t + 1) * 1024)), qf, Z, 0, 0, 0);
        float mx = Sx[0];
#pragma unroll
        for (int i = 1; i < 16; ++i) mx = fmaxf(mx, Sx[i]);
        { auto rr = __builtin_amdgcn_permlane32_swap(__float_as_uint(mx), __float_as_uint(mx), false, false); mx = fmaxf(__uint_as_float(rr[0]), __uint_as_float(rr[1])); }
        if (!__all((mx - mrun) * cs <= 8.0f)) {
            const float mnew = fmaxf(mrun, mx);
            const float alpha = __builtin_amdgcn_exp2f((mrun - mnew) * cs);
#pragma unroll
            for (int i = 0; i < 16; ++i) O[i] *= alpha;
            mrun = mnew;
        }
        const float mc = mrun * cs;
        bf16x8 p0, p1;
#pragma unroll
        for (int i = 0; i < 8; ++i) { p0[i] = (__bf16)__builtin_amdgcn_exp2f(__builtin_fmaf(Sx[i], cs, -mc)); p1[i] = (__bf16)__builtin_amdgcn_exp2f(__builtin_fmaf(Sx[8 + i], cs, -mc)); }
        O = __builtin_amdgcn_mfma_f32_32x32x16_bf16(vf0, p0, O, 0, 0, 0);
        O = __builtin_amdgcn_mfma_f32_32x32x16_bf16(vf1, p1, O, 0, 0, 0);
    }
    __syncthreads();
    float* const mg = (float*)smem;
    if (khalf == 1) {
        float* m = mg + ((w & 7) * 64 + lane) * 10;
#pragma unroll
        for (int i = 0; i < 9; ++i) m[i] = O[i];
        m[9] = mrun;
    }
    __syncthreads();
    if (khalf == 1) return;
    {
        const float* m = mg + ((w & 7) * 64 + lane) * 10;
        const float m2 = m[9], mnew = fmaxf(mrun, m2);
        const float a1 = __builtin_amdgcn_exp2f((mrun - mnew) * cs), a2 = __builtin_amdgcn_exp2f((m2 - mnew) * cs);
#pragma unroll
        for (int i = 0; i < 9; ++i) O[i] = O[i] * a1 + m[i] * a2;
    }
    float lsum = O[8];
    { auto rr = __builtin_amdgcn_permlane32_swap(__float_as_uint(lsum), __float_as_uint(lsum), false, false); lsum = __uint_as_float(rr[0]); }
    const float inv = 1.0f / lsum;
    u32x2 a, b;
    a.x = pk2(O[0] * inv, O[1] * inv); a.y = pk2(O[2] * inv, O[3] * inv);
    b.x = pk2(O[4] * inv, O[5] * inv); b.y = pk2(O[6] * inv, O[7] * inv);
    u16* op = o + (size_t)token * DM + l32 * 16;
    *(u32x2*)(op + hh * 4) = a;
    *(u32x2*)(op + 8 + hh * 4) = b;
}

#define POISON 0xFFFFFFFFu
constexpr int CH_WG = 64;
constexpr int PB_WORDS = 2 * CH_WG * CH_WG * 64;
constexpr int MB_WORDS = 3 * CH_WG * 32;
#define CH_D0A 14
#define CH_D0B 18
__device__ __forceinline__ void ld16_sc1(u32x4& v, const unsigned* p) { asm volatile("global_load_dwordx4 %0, %1, off sc1" : "=&v"(v) : "v"(p) : "memory"); }
__device__ __forceinline__ void st16_sc1(unsigned* p, u32x4 v) { asm volatile("global_store_dwordx4 %0, %1, off sc1\n\ts_nop 1" :: "v"(p), "v"(v) : "memory"); }
__device__ __forceinline__ void st8_sc1(unsigned* p, u32x2 v) { asm volatile("global_store_dwordx2 %0, %1, off sc1\n\ts_nop 1" :: "v"(p), "v"(v) : "memory"); }
__device__ __forceinline__ void st16_plain(void* p, u32x4 v) { asm volatile("global_store_dwordx4 %0, %1, off\n\ts_nop 1" :: "v"(p), "v"(v) : "memory"); }
__device__ __forceinline__ void st8_plain(void* p, u32x2 v) { asm volatile("global_store_dwordx2 %0, %1, off\n\ts_nop 1" :: "v"(p), "v"(v) : "memory"); }
#define ST16_SC1_OFF(p, v, off) asm volatile("global_store_dwordx4 %0, %1, off offset:%2 sc1\n\ts_nop 1" :: "v"(p), "v"(v), "i"(off) : "memory")

__device__ __forceinline__ bool okv(const u32x4& v) { return (v.x != POISON) & (v.y != POISON) & (v.z != POISON) & (v.w != POISON); }
__device__ __forceinline__ unsigned unpoison(float f) { return min(__float_as_uint(f), 0xFFFFFFFEu); }
__device__ __forceinline__ void sleep_units(int n) { for (int i = 0; i < n; ++i) __builtin_amdgcn_s_sleep(1); }
__device__ __forceinline__ float row_sum_mod4(float x) {
    x += __builtin_bit_cast(float, __builtin_amdgcn_update_dpp(0, __builtin_bit_cast(int, x), 0x124, 0xf, 0xf, false));
    x += __builtin_bit_cast(float, __builtin_amdgcn_update_dpp(0, __builtin_bit_cast(int, x), 0x128, 0xf, 0xf, false));
    return xsum16_32(x);
}

__global__ __launch_bounds__(256, 1) void chain_kernel(const u16* __restrict__ WT, const u16* __restrict__ WTo,
                                                        const u16* __restrict__ QK, const u16* __restrict__ VT, const u16* __restrict__ mem0,
                                                        u32x4* __restrict__ KM, u32x2* __restrict__ VM, unsigned* PB, unsigned* MB, unsigned* abortw) {
    __shared__ u32x4 memf[16 * 64];
    __shared__ f32x4 sS[4][3][64];
    __shared__ u32x4 ofr[2][64];
    __shared__ u32x4 wvs[4 * 16 * 64];
    const int tid = threadIdx.x, w = tid >> 6, lane = tid & 63, g = lane >> 4, r16 = lane & 15;
    const int b = blockIdx.x, h = b >> 1, a = b & 1;
    const int u2 = w >> 1, e2 = w & 1;
    typedef __attribute__((address_space(1))) unsigned gu32;
    bf16x8 wq[2][16], wk[2][16], wo[8][2];
#pragma unroll
    for (int tt = 0; tt < 2; ++tt) {
        const int d = 32 * w + 8 * (r16 >> 2) + 4 * tt + (r16 & 3);
        const u16* pq = WT + (size_t)(h * DHB + d) * DM + 8 * g;
        const u16* pk = pq + (size_t)NB * DM;
#pragma unroll
        for (int ks = 0; ks < 16; ++ks) {
            wq[tt][ks] = __builtin_bit_cast(bf16x8, *(const u32x4*)(pq + 32 * ks));
            wk[tt][ks] = __builtin_bit_cast(bf16x8, *(const u32x4*)(pk + 32 * ks));
        }
    }
    const int dvl = 32 * u2 + 8 * (r16 >> 2) + 4 * e2 + (r16 & 3);
    {
        const u16* pv = WT + (size_t)(2 * NB + h * DHB + 64 * a + dvl) * DM + 8 * g;
#pragma unroll
        for (int ks = 0; ks < 16; ++ks) wvs[(w * 16 + ks) * 64 + lane] = *(const u32x4*)(pv + 32 * ks);
    }
#pragma unroll
    for (int t = 0; t < 8; ++t) {
        const u16* po = WTo + (size_t)(128 * w + 16 * t + r16) * NB + h * DHB + 64 * a + 8 * g;
        wo[t][0] = __builtin_bit_cast(bf16x8, *(const u32x4*)(po));
        wo[t][1] = __builtin_bit_cast(bf16x8, *(const u32x4*)(po + 32));
    }
    for (int i = tid; i < 1024; i += 256) memf[i] = (u32x4){0u, 0u, 0u, 0u};
    __syncthreads();
    for (int i = tid; i < 512; i += 256) {
        const int u = i >> 3, m = i & 7;
        memf[(u >> 2) * 64 + (u & 3) * 16 + m] = *(const u32x4*)(mem0 + m * DM + 8 * u);
    }
    const int keyr = 8 * (r16 >> 2) + (r16 & 3);
    const u16* kbase = QK + (size_t)keyr * 8192 + NB + h * DHB + 32 * w + 8 * g;
    const u16* vbase = VT + (size_t)(h * DHB + 64 * a + dvl) * S_LEN + 8 * g;
    u32x4 kt0 = *(const u32x4*)(kbase), kt1 = *(const u32x4*)(kbase + (size_t)4 * 8192), vt = *(const u32x4*)(vbase);
    __syncthreads();
    u32x4 ktn0 = {0u,0u,0u,0u}, ktn1 = ktn0, vtn = ktn0;

    const float cs = 0.08838834764831845f * 1.4426950408889634f;
    const f32x4 z4 = {0.f, 0.f, 0.f, 0.f};
    unsigned* const pub_lane = PB + ((size_t)b * CH_WG + 16 * w + (g >> 1)) * 64 + (g & 1) * 32 + r16 * 4;
    const unsigned* const gat_lane = PB + ((size_t)(lane >> 2) * CH_WG + b) * 64 + (4 * w + (lane & 3)) * 4;
    bool dead = false;
    for (int s = 0; s < NSEG; ++s) {
        if (s + 1 < NSEG) {
            asm volatile("global_load_dwordx4 %0, %3, off\n\tglobal_load_dwordx4 %1, %4, off\n\tglobal_load_dwordx4 %2, %5, off"
                         : "=&v"(ktn0), "=&v"(ktn1), "=&v"(vtn)
                         : "v"(kbase + (size_t)(s + 1) * SEG * 8192), "v"(kbase + (size_t)((s + 1) * SEG + 4) * 8192), "v"(vbase + (size_t)(s + 1) * SEG) : "memory");
        }
        f32x4 aq0, aq1, ak0, ak1, av;
        {
            bf16x8 mfr[3], wvr[3];
            mfr[0] = __builtin_bit_cast(bf16x8, memf[lane]);
            wvr[0] = __builtin_bit_cast(bf16x8, wvs[(w * 16) * 64 + lane]);
            mfr[1] = __builtin_bit_cast(bf16x8, memf[64 + lane]);
            wvr[1] = __builtin_bit_cast(bf16x8, wvs[(w * 16 + 1) * 64 + lane]);
#pragma unroll
            for (int ks = 0; ks < 16; ++ks) {
                if (ks + 2 < 16) {
                    mfr[(ks + 2) % 3] = __builtin_bit_cast(bf16x8, memf[(ks + 2) * 64 + lane]);
                    wvr[(ks + 2) % 3] = __builtin_bit_cast(bf16x8, wvs[(w * 16 + ks + 2) * 64 + lane]);
                }
                if (ks == 0) {
                    asm volatile("s_nop 1\n\tv_mfma_f32_16x16x32_bf16 %0, %5, %9, 0\n\t"
                                 "v_mfma_f32_16x16x32_bf16 %1, %6, %9, 0\n\t"
                                 "v_mfma_f32_16x16x32_bf16 %2, %7, %9, 0\n\t"
                                 "v_mfma_f32_16x16x32_bf16 %3, %8, %9, 0\n\t"
                                 "v_mfma_f32_16x16x32_bf16 %4, %9, %10, 0"
                                 : "=&v"(aq0), "=&v"(aq1), "=&v"(ak0), "=&v"(ak1), "=&v"(av)
                                 : "a"(wq[0][ks]), "a"(wq[1][ks]), "a"(wk[0][ks]), "a"(wk[1][ks]), "v"(mfr[ks % 3]), "v"(wvr[ks % 3]));
                } else if (ks < 15) {
                    asm volatile("s_nop 1\n\tv_mfma_f32_16x16x32_bf16 %0, %5, %9, %0\n\t"
                                 "v_mfma_f32_16x16x32_bf16 %1, %6, %9, %1\n\t"
                                 "v_mfma_f32_16x16x32_bf16 %2, %7, %9, %2\n\t"
                                 "v_mfma_f32_16x16x32_bf16 %3, %8, %9, %3\n\t"
                                 "v_mfma_f32_16x16x32_bf16 %4, %9, %10, %4"
                                 : "+v"(aq0), "+v"(aq1), "+v"(ak0), "+v"(ak1), "+v"(av)
                                 : "a"(wq[0][ks]), "a"(wq[1][ks]), "a"(wk[0][ks]), "a"(wk[1][ks]), "v"(mfr[ks % 3]), "v"(wvr[ks % 3]));
                } else {
                    asm volatile("s_nop 1\n\tv_mfma_f32_16x16x32_bf16 %0, %5, %9, %0\n\t"
                                 "v_mfma_f32_16x16x32_bf16 %1, %6, %9, %1\n\t"
                                 "v_mfma_f32_16x16x32_bf16 %2, %7, %9, %2\n\t"
                                 "v_mfma_f32_16x16x32_bf16 %3, %8, %9, %3\n\t"
                                 "v_mfma_f32_16x16x32_bf16 %4, %9, %10, %4\n\t"
                                 "s_nop 7"
                                 : "+v"(aq0), "+v"(aq1), "+v"(ak0), "+v"(ak1), "+v"(av)
                                 : "a"(wq[0][ks]), "a"(wq[1][ks]), "a"(wk[0][ks]), "a"(wk[1][ks]), "v"(mfr[ks % 3]), "v"(wvr[ks % 3]));
                }
            }
        }
        bf16x8 qf, kmf;
#pragma unroll
        for (int i = 0; i < 4; ++i) { qf[i] = (__bf16)aq0[i]; qf[4 + i] = (__bf16)aq1[i]; kmf[i] = (__bf16)ak0[i]; kmf[4 + i] = (__bf16)ak1[i]; }
        sS[w][0][lane] = __builtin_amdgcn_mfma_f32_16x16x32_bf16(__builtin_bit_cast(bf16x8, kt0), qf, z4, 0, 0, 0);
        sS[w][1][lane] = __builtin_amdgcn_mfma_f32_16x16x32_bf16(__builtin_bit_cast(bf16x8, kt1), qf, z4, 0, 0, 0);
        sS[w][2][lane] = __builtin_amdgcn_mfma_f32_16x16x32_bf16(kmf, qf, z4, 0, 0, 0);
        LDS_BARRIER();
        float sc[12];
        {
            f32x4 pp[4][3];
#pragma unroll
            for (int ww = 0; ww < 4; ++ww)
#pragma unroll
                for (int tI = 0; tI < 3; ++tI) pp[ww][tI] = sS[ww][tI][lane];
#pragma unroll
            for (int tI = 0; tI < 3; ++tI)
#pragma unroll
                for (int i = 0; i < 4; ++i) sc[tI * 4 + i] = ((pp[0][tI][i] + pp[1][tI][i]) + pp[2][tI][i]) + pp[3][tI][i];
        }
        if (g >= 2) { sc[8] = -INFINITY; sc[9] = -INFINITY; sc[10] = -INFINITY; sc[11] = -INFINITY; }
        float mx = sc[0];
#pragma unroll
        for (int i = 1; i < 12; ++i) mx = fmaxf(mx, sc[i]);
        mx = xmax16_32(mx);
        const float mc = mx * cs;
        float sum = 0.f;
#pragma unroll
        for (int i = 0; i < 12; ++i) { sc[i] = __builtin_amdgcn_exp2f(__builtin_fmaf(sc[i], cs, -mc)); sum += sc[i]; }
        sum = xsum16_32(sum);
        const float inv = 1.0f / sum;
        bf16x8 pf0, pf1, vmf;
#pragma unroll
        for (int i = 0; i < 4; ++i) { pf0[i] = (__bf16)(sc[i] * inv); pf0[4 + i] = (__bf16)(sc[4 + i] * inv); pf1[i] = (__bf16)(sc[8 + i] * inv); pf1[4 + i] = (__bf16)0.f; vmf[i] = (__bf16)av[i]; vmf[4 + i] = (__bf16)0.f; }
        f32x4 ao = __builtin_amdgcn_mfma_f32_16x16x32_bf16(__builtin_bit_cast(bf16x8, vt), pf0, z4, 0, 0, 0);
        ao = __builtin_amdgcn_mfma_f32_16x16x32_bf16(vmf, pf1, ao, 0, 0, 0);
        ((u32x2*)&ofr[u2][lane])[e2] = (u32x2){pk2(ao[0], ao[1]), pk2(ao[2], ao[3])};
        LDS_BARRIER();
        const bf16x8 of0 = __builtin_bit_cast(bf16x8, ofr[0][lane]), of1 = __builtin_bit_cast(bf16x8, ofr[1][lane]);
        const int par = s & 1;
        {
            f32x4 oc[8];
#pragma unroll
            for (int t = 0; t < 8; ++t) { oc[t] = __builtin_amdgcn_mfma_f32_16x16x32_bf16(wo[t][0], of0, z4, 0, 0, 0); }
#pragma unroll
            for (int t = 0; t < 8; ++t) { oc[t] = __builtin_amdgcn_mfma_f32_16x16x32_bf16(wo[t][1], of1, oc[t], 0, 0, 0); }
            if (r16 < 8) {
                unsigned* pp = pub_lane + (size_t)par * CH_WG * CH_WG * 64;
#pragma unroll
                for (int t = 0; t < 8; ++t) {
                    const u32x4 v = {unpoison(oc[t][0]), unpoison(oc[t][1]), unpoison(oc[t][2]), unpoison(oc[t][3])};
                    ST16_SC1_OFF(pp, v, t * 512);
                }
            }
        }
        if (a == 0) st16_plain(KM + ((size_t)(s * HB + h) * 4 + w) * 64 + lane, __builtin_bit_cast(u32x4, kmf));
        { const u32x4 vm4 = __builtin_bit_cast(u32x4, vmf); st8_plain(VM + (((size_t)(s * HB + h) * 2 + a) * 4 + w) * 64 + lane, (u32x2){vm4.x, vm4.y}); }
        float r0 = 0.f, r1 = 0.f, r2 = 0.f, r3 = 0.f;
        const unsigned* gb = gat_lane + (size_t)par * CH_WG * CH_WG * 64;
        {
            sleep_units(CH_D0A);
            unsigned spins = 0;
            for (;;) {
                u32x4 v[4];
#pragma unroll
                for (int j = 0; j < 4; ++j) ld16_sc1(v[j], gb + (size_t)j * 16 * CH_WG * 64);
                WAITV();
                bool ok = true;
#pragma unroll
                for (int j = 0; j < 4; ++j) { asm volatile("" : "+v"(v[j])); ok &= okv(v[j]); }
                if (__all(ok)) {
#pragma unroll
                    for (int j = 0; j < 4; ++j) { r0 += __uint_as_float(v[j].x); r1 += __uint_as_float(v[j].y); r2 += __uint_as_float(v[j].z); r3 += __uint_as_float(v[j].w); }
                    break;
                }
                if (++spins > (1u << 18)) { __hip_atomic_store((gu32*)abortw, 1u, __ATOMIC_RELAXED, __HIP_MEMORY_SCOPE_AGENT); dead = true; break; }
                if ((spins & 63) == 0 && __hip_atomic_load((gu32*)abortw, __ATOMIC_RELAXED, __HIP_MEMORY_SCOPE_AGENT)) { dead = true; break; }
            }
        }
        asm volatile("" : "+v"(ktn0), "+v"(ktn1), "+v"(vtn));
        r0 = row_sum_mod4(r0); r1 = row_sum_mod4(r1); r2 = row_sum_mod4(r2); r3 = row_sum_mod4(r3);
        {
            const int q = 4 * w + (lane & 3), h2 = q >> 3, m = q & 7;
            if (lane < 4) {
                unsigned w0 = pk2(r0, r1), w1 = pk2(r2, r3);
                w0 = min(w0, 0xFFFFFFFEu); w1 = min(w1, 0xFFFFFFFEu);
                st8_sc1(MB + ((size_t)(s % 3) * CH_WG + b) * 32 + m * 4 + h2 * 2, (u32x2){w0, w1});
            } else if (lane < 8 && s >= 1) {
                st8_sc1(MB + ((size_t)((s + 2) % 3) * CH_WG + b) * 32 + m * 4 + h2 * 2, (u32x2){POISON, POISON});
            }
        }
        {
            const u32x4 pz = {POISON, POISON, POISON, POISON};
#pragma unroll
            for (int j = 0; j < 4; ++j) st16_sc1((unsigned*)gb + (size_t)j * 16 * CH_WG * 64, pz);
        }
        if (dead) break;
        kt0 = ktn0; kt1 = ktn1; vt = vtn;
        {
            const unsigned* mbb = MB + (size_t)(s % 3) * CH_WG * 32 + tid * 4;
            sleep_units(CH_D0B);
            unsigned spins = 0;
            for (;;) {
                u32x4 v[2];
                ld16_sc1(v[0], mbb); ld16_sc1(v[1], mbb + 1024);
                WAITV();
                asm volatile("" : "+v"(v[0])); asm volatile("" : "+v"(v[1]));
                if (__all(okv(v[0]) & okv(v[1]))) {
                    const int i0 = tid, i1 = tid + 256;
                    memf[((i0 >> 3) >> 2) * 64 + ((i0 >> 3) & 3) * 16 + (i0 & 7)] = v[0];
                    memf[((i1 >> 3) >> 2) * 64 + ((i1 >> 3) & 3) * 16 + (i1 & 7)] = v[1];
                    break;
                }
                if (++spins > (1u << 18)) { __hip_atomic_store((gu32*)abortw, 1u, __ATOMIC_RELAXED, __HIP_MEMORY_SCOPE_AGENT); dead = true; break; }
                if ((spins & 63) == 0 && __hip_atomic_load((gu32*)abortw, __ATOMIC_RELAXED, __HIP_MEMORY_SCOPE_AGENT)) { dead = true; break; }
            }
        }
        LDS_BARRIER();
        if (dead) break;
    }
}

__global__ __launch_bounds__(256) void tokattn_kernel(const u16* __restrict__ QK, const u16* __restrict__ VT, const u32x4* __restrict__ KM,
                                                       const u32x2* __restrict__ VM, u16* __restrict__ o) {
    const int tid = threadIdx.x, w = tid >> 6, lane = tid & 63, g = lane >> 4, r16 = lane & 15;
    const int pair = blockIdx.x * 4 + w, s = pair >> 5, h = pair & 31;
    const float cs = 0.08838834764831845f * 1.4426950408889634f;
    const int keyr = 8 * (r16 >> 2) + (r16 & 3);
    const u16* qp = QK + (size_t)(s * SEG + r16) * 8192 + h * DHB + 8 * g;
    const u16* kp = QK + (size_t)(s * SEG + keyr) * 8192 + NB + h * DHB + 8 * g;
    const f32x4 z4 = {0.f, 0.f, 0.f, 0.f};
    f32x4 St[2][3];
#pragma unroll
    for (int qt = 0; qt < 2; ++qt)
#pragma unroll
        for (int kt = 0; kt < 3; ++kt) St[qt][kt] = z4;
#pragma unroll
    for (int ks = 0; ks < 4; ++ks) {
        const bf16x8 q0 = __builtin_bit_cast(bf16x8, *(const u32x4*)(qp + 32 * ks));
        const bf16x8 q1 = __builtin_bit_cast(bf16x8, *(const u32x4*)(qp + (size_t)16 * 8192 + 32 * ks));
        const bf16x8 k0 = __builtin_bit_cast(bf16x8, *(const u32x4*)(kp + 32 * ks));
        const bf16x8 k1 = __builtin_bit_cast(bf16x8, *(const u32x4*)(kp + (size_t)4 * 8192 + 32 * ks));
        const bf16x8 km = __builtin_bit_cast(bf16x8, KM[((size_t)(s * HB + h) * 4 + ks) * 64 + lane]);
        St[0][0] = __builtin_amdgcn_mfma_f32_16x16x32_bf16(k0, q0, St[0][0], 0, 0, 0);
        St[0][1] = __builtin_amdgcn_mfma_f32_16x16x32_bf16(k1, q0, St[0][1], 0, 0, 0);
        St[0][2] = __builtin_amdgcn_mfma_f32_16x16x32_bf16(km, q0, St[0][2], 0, 0, 0);
        St[1][0] = __builtin_amdgcn_mfma_f32_16x16x32_bf16(k0, q1, St[1][0], 0, 0, 0);
        St[1][1] = __builtin_amdgcn_mfma_f32_16x16x32_bf16(k1, q1, St[1][1], 0, 0, 0);
        St[1][2] = __builtin_amdgcn_mfma_f32_16x16x32_bf16(km, q1, St[1][2], 0, 0, 0);
    }
    bf16x8 pf[2][2];
#pragma unroll
    for (int qt = 0; qt < 2; ++qt) {
        const int qi = 16 * qt + r16;
        float sc[12];
#pragma unroll
        for (int i = 0; i < 4; ++i) {
            sc[i] = (8 * g + i <= qi) ? St[qt][0][i] : -INFINITY;
            sc[4 + i] = (8 * g + 4 + i <= qi) ? St[qt][1][i] : -INFINITY;
            sc[8 + i] = (g < 2) ? St[qt][2][i] : -INFINITY;
        }
        float mx = sc[0];
#pragma unroll
        for (int i = 1; i < 12; ++i) mx = fmaxf(mx, sc[i]);
        mx = xmax16_32(mx);
        const float mc = mx * cs;
        float sum = 0.f;
#pragma unroll
        for (int i = 0; i < 12; ++i) { sc[i] = __builtin_amdgcn_exp2f(__builtin_fmaf(sc[i], cs, -mc)); sum += sc[i]; }
        sum = xsum16_32(sum);
        const float inv = 1.0f / sum;
#pragma unroll
        for (int i = 0; i < 4; ++i) { pf[qt][0][i] = (__bf16)(sc[i] * inv); pf[qt][0][4 + i] = (__bf16)(sc[4 + i] * inv); pf[qt][1][i] = (__bf16)(sc[8 + i] * inv); pf[qt][1][4 + i] = (__bf16)0.f; }
    }
#pragma unroll
    for (int a = 0; a < 2; ++a)
#pragma unroll
        for (int wv = 0; wv < 4; ++wv) {
            const int dl = 32 * (wv >> 1) + 8 * (r16 >> 2) + 4 * (wv & 1) + (r16 & 3);
            const bf16x8 vtf = __builtin_bit_cast(bf16x8, *(const u32x4*)(VT + (size_t)(h * DHB + 64 * a + dl) * S_LEN + s * SEG + 8 * g));
            const u32x2 vm2 = VM[(((size_t)(s * HB + h) * 2 + a) * 4 + wv) * 64 + lane];
            const bf16x8 vmf = __builtin_bit_cast(bf16x8, (u32x4){vm2.x, vm2.y, 0u, 0u});
#pragma unroll
            for (int qt = 0; qt < 2; ++qt) {
                f32x4 ao = __builtin_amdgcn_mfma_f32_16x16x32_bf16(vtf, pf[qt][0], z4, 0, 0, 0);
                ao = __builtin_amdgcn_mfma_f32_16x16x32_bf16(vmf, pf[qt][1], ao, 0, 0, 0);
                u16* op = o + (size_t)(s * SEG + 16 * qt + r16) * NB + h * DHB + 64 * a + 32 * (wv >> 1) + 8 * g + 4 * (wv & 1);
                *(u32x2*)op = (u32x2){pk2(ao[0], ao[1]), pk2(ao[2], ao[3])};
            }
        }
}

}

extern "C" void kernel_launch(void* const* d_in, const int* in_sizes, int n_in,
                              void* d_out, int out_size, void* d_ws, size_t ws_size,
                              hipStream_t stream) {
    const u16* A_x = (const u16*)d_in[0];
    const u16* A_wq = (const u16*)d_in[1];
    const u16* A_wk = (const u16*)d_in[2];
    const u16* A_wv = (const u16*)d_in[3];
    const u16* A_wo = (const u16*)d_in[4];
    const u16* B_mem0 = (const u16*)d_in[6];
    const u16* B_Wq = (const u16*)d_in[7];
    const u16* B_Wk = (const u16*)d_in[8];
    const u16* B_Wv = (const u16*)d_in[9];
    const u16* B_Wo = (const u16*)d_in[10];
    u16* out = (u16*)d_out;

    unsigned char* ws = (unsigned char*)d_ws;
    size_t off = 0;
    auto take = [&](size_t bytes) { unsigned char* p = ws + off; off += (bytes + 255) & ~(size_t)255; return p; };
    u16* wtB = (u16*)take((size_t)3 * NB * DM * 2);
    u16* wtoB = (u16*)take((size_t)DM * NB * 2);
    u16* wtA = (u16*)take((size_t)QKVA_LD * DM * 2);
    u16* wtoA = (u16*)take((size_t)DM * DM * 2);
    u16* xa = (u16*)take((size_t)S_LEN * DM * 2);
    u16* xb = (u16*)take((size_t)S_LEN * DM * 2);
    u16* xn = (u16*)take((size_t)S_LEN * DM * 2);
    u16* qkvA = (u16*)take((size_t)S_LEN * QKVA_LD * 2);
    u16* oA = (u16*)take((size_t)S_LEN * DM * 2);
    u16* QK = (u16*)take((size_t)S_LEN * 8192 * 2);
    u16* VT = (u16*)take((size_t)NB * S_LEN * 2);
    u16* oB = (u16*)take((size_t)S_LEN * NB * 2);
    u32x4* KM = (u32x4*)take((size_t)NSEG * HB * 4 * 64 * 16);
    u32x2* VM = (u32x2*)take((size_t)NSEG * HB * 2 * 4 * 64 * 8);
    unsigned* xchg = (unsigned*)take((size_t)(PB_WORDS + MB_WORDS + 64) * 4);
    unsigned* PB = xchg;
    unsigned* MB = xchg + PB_WORDS;
    unsigned* abortw = (unsigned*)take(256);
    const int xchg_vec = (PB_WORDS + MB_WORDS) / 4;

    hipMemsetAsync(abortw, 0, 256, stream);
    prep_kernel<<<2193, 256, 0, stream>>>(B_Wq, B_Wk, B_Wv, B_Wo, A_wq, A_wk, A_wv, A_wo, wtB, wtoB, wtA, wtoA);

    const int attnA_lds = 65536 + 16 * AVROW + 128;
    hipFuncSetAttribute((const void*)attnA_kernel, hipFuncAttributeMaxDynamicSharedMemorySize, attnA_lds);
    hipFuncSetAttribute((const void*)gemm256_kernel, hipFuncAttributeMaxDynamicSharedMemorySize, g256::SHM_B);

    const u16* xin = A_x;
    u16* xouts[4] = {xa, xb, xa, out};
    for (int layer = 0; layer < 4; ++layer) {
        u16* xo = xouts[layer];
        if ((layer & 1) == 0) {
            ln_kernel<<<S_LEN / 4, 256, 0, stream>>>(xin, xn, nullptr, 0);
            gemm_nt<128, 128, 0><<<(S_LEN / 128) * (QKVA_LD / 128), 256, 0, stream>>>(xn, DM, wtA, DM, qkvA, QKVA_LD, nullptr, 0, DM, QKVA_LD / 128);
            attnA_kernel<<<S_LEN / 8, 1024, attnA_lds, stream>>>(qkvA, oA);
            gemm_nt<64, 64, 1><<<(S_LEN / 64) * (DM / 64), 256, 0, stream>>>(oA, DM, wtoA, DM, xo, DM, xin, DM, DM, DM / 64);
        } else {
            ln_kernel<<<S_LEN / 4, 256, 0, stream>>>(xin, xn, (u32x4*)xchg, xchg_vec);
            gemm256_kernel<<<(S_LEN / 256) * (8192 / 256), 512, g256::SHM_B, stream>>>(xn, wtB, QK, S_LEN, 8192, DM, 8192);
            gemm_nt<128, 128, 0><<<(NB / 128) * (S_LEN / 128), 256, 0, stream>>>(wtB + (size_t)2 * NB * DM, DM, xn, DM, VT, S_LEN, nullptr, 0, DM, S_LEN / 128);
            chain_kernel<<<CH_WG, 256, 0, stream>>>(wtB, wtoB, QK, VT, B_mem0, KM, VM, PB, MB, abortw);
            tokattn_kernel<<<NSEG * HB / 4, 256, 0, stream>>>(QK, VT, KM, VM, oB);
            gemm_nt<64, 64, 1><<<(S_LEN / 64) * (DM / 64), 256, 0, stream>>>(oB, NB, wtoB, NB, xo, DM, xin, DM, NB, DM / 64);
        }
        xin = xo;
    }
}
```
